```python
import torch

def _softmax(x, axis=-1):
    return torch.softmax(x, dim=axis)
def _gelu(x):
    return torch.nn.functional.gelu(x, approximate="tanh")
def _silu(x):
    return torch.nn.functional.silu(x)
def _sigmoid(x):
    return torch.sigmoid(x)
def _layernorm(x, axis=-1, eps=1e-5):
    m = x.mean(dim=axis, keepdim=True)
    v = x.var(dim=axis, keepdim=True, unbiased=False)
    return (x - m) / torch.sqrt(v + eps)
def _scatter_add(tgt, idx, src, axis=0):
    return tgt.clone().index_add_(axis, idx, src)
def _segment_sum(src, seg, n):
    out = torch.zeros((n,) + src.shape[1:], dtype=src.dtype)
    return out.index_add_(0, seg, src)
def _pad(x, pw):
    flat = tuple(v for pair in reversed(pw) for v in pair)
    return torch.nn.functional.pad(x, flat)

kernel_name = 'recurrent_scan__softmax_full_mha__mixer_s_mlp__prompt'

def setup_inputs():

    _w = (lambda a: (lambda t: t.to(torch.bfloat16) if t.is_floating_point() else t)(torch.as_tensor(a)))
    def _A_setup():
        B, S, D = 1, 2048, 576
        return {
            "x": _w((lambda *s: torch.randn(*s, dtype=torch.bfloat16) * 0.05)(B, S, D)),
            "wqkv": _w((lambda *s: torch.randn(*s, dtype=torch.bfloat16) * 0.05)(D, 3 * D)),
            "wo": _w((lambda *s: torch.randn(*s, dtype=torch.bfloat16) * 0.05)(D, D)),
        }
    _A_in = _A_setup()

    def _B_setup():
        B, S, D, F = 1, 2048, 576, 4608
        return {
            "x":  _w((lambda *s: torch.randn(*s, dtype=torch.bfloat16) * 0.05)(B, S, D)),
            "w1": _w((lambda *s: torch.randn(*s, dtype=torch.bfloat16) * 3.2)(D, D) * (D ** -0.5)),
            "w2": _w((lambda *s: torch.randn(*s, dtype=torch.bfloat16) * 3.2)(D, D) * (D ** -0.5)),
            "w3": _w((lambda *s: torch.randn(*s, dtype=torch.bfloat16) * 3.2)(D, F) * (D ** -0.5)),
            "w4": _w((lambda *s: torch.randn(*s, dtype=torch.bfloat16) * 3.2)(F, D) * (F ** -0.5)),
        }
    _B_in = _B_setup()

    return {
        "A_x": _A_in["x"],
        "A_wqkv": _A_in["wqkv"],
        "A_wo": _A_in["wo"],
        "B_x": _B_in["x"],
        "B_w1": _B_in["w1"],
        "B_w2": _B_in["w2"],
        "B_w3": _B_in["w3"],
        "B_w4": _B_in["w4"],
    }

def reference(A_x, A_wqkv, A_wo, B_x, B_w1, B_w2, B_w3, B_w4):
    def _A_ref(x, wqkv, wo):
        B, S, D = x.shape
        H = 32; Dh = D // H
        qkv = (lambda x, *d: x.permute(*d))((x @ wqkv).reshape(B, S, 3, H, Dh), 2, 0, 3, 1, 4)
        q, k, v = qkv[0], qkv[1], qkv[2]
        scale = Dh ** -0.5
        scores = torch.einsum("bhqd,bhkd->bhqk", q, k) * scale
        p = _softmax(scores, axis=-1)
        o = torch.einsum("bhqk,bhkd->bhqd", p, v)
        return (lambda x, *d: x.permute(*d))(o, 0, 2, 1, 3).reshape(B, S, D) @ wo

    def _B_ref(x, w1, w2, w3, w4):
        B, S, D = x.shape
        h = _gelu(_layernorm(x, axis=-1) @ w1)
        c = D // 4
        g0 = _pad(h[:, :, 0*c:1*c], ((0, 0), (1, 0), (0, 0)))[:, :S, :]
        g1 = _pad(h[:, :, 1*c:2*c], ((0, 0), (0, 1), (0, 0)))[:, 1:, :]
        g2 = _pad(h[:, :, 2*c:3*c], ((0, 0), (2, 0), (0, 0)))[:, :S, :]
        g3 = _pad(h[:, :, 3*c:   ], ((0, 0), (0, 2), (0, 0)))[:, 2:, :]
        hs = torch.concatenate([g0, g1, g2, g3], axis=-1) @ w2
        x1 = x + hs
        h2 = _gelu(_layernorm(x1, axis=-1) @ w3) @ w4
        return x1 + h2

    x = A_x
    xn = _layernorm(x, axis=-1)
    s = _A_ref(xn, A_wqkv, A_wo)
    g = _silu(_B_ref(xn, B_w1, B_w2, B_w3, B_w4))
    return x + s * g
```

```cpp
#include <hip/hip_runtime.h>
#include <hip/hip_bf16.h>
#include <stdint.h>

typedef __bf16 bf16;
typedef __bf16 bf16x2 __attribute__((ext_vector_type(2)));
typedef __bf16 bf16x4 __attribute__((ext_vector_type(4)));
typedef __bf16 bf16x8 __attribute__((ext_vector_type(8)));
typedef short s16x4 __attribute__((ext_vector_type(4)));
typedef float f32x2 __attribute__((ext_vector_type(2)));
typedef float f32x4 __attribute__((ext_vector_type(4)));
typedef float f32x16 __attribute__((ext_vector_type(16)));
typedef unsigned u32x2 __attribute__((ext_vector_type(2)));
typedef unsigned u32x4 __attribute__((ext_vector_type(4)));
typedef u32x2 u32x2_a4 __attribute__((aligned(4)));
#define LDS_AS __attribute__((address_space(3)))
#define GLB_AS __attribute__((address_space(1)))
#define DI __device__ __forceinline__

constexpr int S_ = 2048, D_ = 576, F_ = 4608, NH = 32, DH = 18;
constexpr float LN_EPS = 1e-5f;
constexpr float QSCALE = 0.23570226039551584f * 1.4426950408889634f;

constexpr size_t WS_STAT = 0;
constexpr size_t WS_KMAX = WS_STAT + 2 * S_ * 4;
constexpr size_t WS_Q    = WS_KMAX + 32 * 16 * 4;
constexpr int    QRT_B   = 4608;
constexpr int    KVBLK_B = 18944;
constexpr int    KVHEAD_B = 8 * KVBLK_B;
constexpr size_t WS_KV   = WS_Q + (size_t)NH * 16 * QRT_B;
constexpr size_t WS_H    = WS_KV + (size_t)NH * KVHEAD_B;
constexpr size_t WS_X1   = WS_H + (size_t)(S_ + 4) * D_ * 2;
constexpr size_t WS_O    = WS_X1 + (size_t)S_ * D_ * 2;
constexpr size_t WS_HH   = WS_O + (size_t)S_ * D_ * 2;
constexpr size_t WS_PART = WS_HH + (size_t)S_ * F_ * 2;
constexpr size_t WS_END  = WS_PART + (size_t)4 * S_ * D_ * 4;

DI unsigned pack2(float lo, float hi) { f32x2 v = {lo, hi}; bf16x2 b = __builtin_convertvector(v, bf16x2); return __builtin_bit_cast(unsigned, b); }
DI float bf_lo(unsigned u) { return __builtin_bit_cast(float, u << 16); }
DI float bf_hi(unsigned u) { return __builtin_bit_cast(float, u & 0xffff0000u); }
DI float gelu_tanh(float x) {
  const float c0 = -2.0f * 0.7978845608028654f * 1.4426950408889634f;
  const float c1 = c0 * 0.044715f;
  float t = x * x;
  float a = x * __builtin_fmaf(t, c1, c0);
  float e = __builtin_amdgcn_exp2f(a);
  return x * __builtin_amdgcn_rcpf(1.0f + e);
}
DI void dma16(const void* g, char* lds) {
  __builtin_amdgcn_global_load_lds((const GLB_AS void*)g, (LDS_AS void*)lds, 16, 0, 0);
}
template <int N> DI void wait_vm() { asm volatile("s_waitcnt vmcnt(%0)" ::"n"(N) : "memory"); }
DI void barrier() { asm volatile("s_waitcnt lgkmcnt(0)\n\ts_barrier" ::: "memory"); }

template <int BM> struct G {
  static constexpr int NT = 9;
  static constexpr int XB = BM * 128, WB = 64 * 288, STAGE = XB + WB, NSTAGE = 4;
  static constexpr int XP = BM / 8;
  static constexpr int XPW = (XP + 3) / 4;
  static constexpr int WPW = 5;
  static constexpr int L = XPW + WPW;
  static constexpr int MT = BM >= 64 ? BM / 64 : 1;
  static constexpr int NTW = BM >= 64 ? 9 : 5;
};

template <int BM, bool TRANS, bool STATS, typename XSRC>
DI void gemm_core(f32x4 (&acc)[G<BM>::MT][G<BM>::NTW], float (&s1)[G<BM>::MT], float (&s2)[G<BM>::MT], float (&cs)[3],
                  XSRC xsrc, const bf16* __restrict__ W, int ldw, int n0, int k0, int nk, char* smem) {
  using g = G<BM>;
  const int tid = threadIdx.x, lane = tid & 63, w = __builtin_amdgcn_readfirstlane(tid >> 6);
  const int r16 = lane & 15, q4 = lane >> 4;
  int woff[g::WPW];
#pragma unroll
  for (int i = 0; i < g::WPW; ++i) {
    int p = w + 4 * i; p = p > 17 ? 17 : p;
    int q = p * 64 + lane, rho = q / 18, cu = q - rho * 18;
    int k = (rho & ~12) | ((rho & 4) << 1) | ((rho & 8) >> 1);
    woff[i] = k * ldw + cu * 8;
  }
  auto issue = [&](int s) {
    char* st = smem + (s & 3) * g::STAGE;
    const int kb = k0 + s * 64;
#pragma unroll
    for (int i = 0; i < g::XPW; ++i) {
      int p = (g::XP >= 4) ? w * g::XPW + i : w; p = p >= g::XP ? g::XP - 1 : p;
      int row = p * 8 + (lane >> 3), u = (lane & 7) ^ ((row >> 1) & 7);
      dma16(xsrc(row, kb + u * 8), st + p * 1024);
    }
#pragma unroll
    for (int i = 0; i < g::WPW; ++i) {
      int p = w + 4 * i; p = p > 17 ? 17 : p;
      dma16(W + (size_t)kb * ldw + n0 + woff[i], st + g::XB + p * 1024);
    }
  };
  const int wrow0 = BM >= 64 ? w * 16 * g::MT : (w & 1) * 16;
  const int jt0 = BM >= 64 ? 0 : (w >> 1) * 4;
  int xaddr[g::MT][2];
#pragma unroll
  for (int i = 0; i < g::MT; ++i)
#pragma unroll
    for (int ks = 0; ks < 2; ++ks) { int row = wrow0 + 16 * i + r16; xaddr[i][ks] = row * 128 + (((4 * ks + q4) ^ ((row >> 1) & 7)) << 4); }
  const int wbase = g::XB + (16 * (q4 >> 1) + 4 * (q4 & 1) + (r16 >> 2)) * 288 + (lane & 3) * 8 + jt0 * 32;
#pragma unroll
  for (int i = 0; i < g::MT; ++i) { s1[i] = 0.f; s2[i] = 0.f;
#pragma unroll
    for (int j = 0; j < g::NTW; ++j) acc[i][j] = f32x4{0, 0, 0, 0}; }
  cs[0] = cs[1] = cs[2] = 0.f;
  issue(0); if (nk > 1) issue(1); if (nk > 2) issue(2);
  for (int s = 0; s < nk; ++s) {
    int rem = nk - 1 - s;
    if (rem >= 2) wait_vm<2 * g::L>(); else if (rem == 1) wait_vm<g::L>(); else wait_vm<0>();
    barrier();
    if (s + 3 < nk) issue(s + 3);
    const char* st = smem + (s & 3) * g::STAGE;
#pragma unroll
    for (int ks = 0; ks < 2; ++ks) {
      bf16x8 xf[g::MT];
#pragma unroll
      for (int i = 0; i < g::MT; ++i) xf[i] = *(const bf16x8*)(st + xaddr[i][ks]);
      if (STATS) {
        const bf16x2 one = {(bf16)1.0f, (bf16)1.0f};
#pragma unroll
        for (int i = 0; i < g::MT; ++i)
#pragma unroll
          for (int e = 0; e < 4; ++e) { bf16x2 p = {xf[i][2 * e], xf[i][2 * e + 1]};
            s1[i] = __builtin_amdgcn_fdot2_f32_bf16(p, one, s1[i], false); s2[i] = __builtin_amdgcn_fdot2_f32_bf16(p, p, s2[i], false); }
      }
#pragma unroll
      for (int j = 0; j < g::NTW; ++j) {
        s16x4 lo = __builtin_amdgcn_ds_read_tr16_b64_v4i16((LDS_AS s16x4*)(st + wbase + (32 * ks) * 288 + j * 32));
        s16x4 hi = __builtin_amdgcn_ds_read_tr16_b64_v4i16((LDS_AS s16x4*)(st + wbase + (32 * ks + 8) * 288 + j * 32));
        bf16x8 wf = __builtin_bit_cast(bf16x8, __builtin_shufflevector(lo, hi, 0, 1, 2, 3, 4, 5, 6, 7));
        if (STATS && BM >= 64) {
          if ((j & 3) == w) {
            const bf16x2 one = {(bf16)1.0f, (bf16)1.0f};
#pragma unroll
            for (int e = 0; e < 4; ++e) { bf16x2 p = {wf[2 * e], wf[2 * e + 1]}; cs[j >> 2] = __builtin_amdgcn_fdot2_f32_bf16(p, one, cs[j >> 2], false); }
          }
        }
#pragma unroll
        for (int i = 0; i < g::MT; ++i)
          acc[i][j] = TRANS ? __builtin_amdgcn_mfma_f32_16x16x32_bf16(wf, xf[i], acc[i][j], 0, 0, 0)
                            : __builtin_amdgcn_mfma_f32_16x16x32_bf16(xf[i], wf, acc[i][j], 0, 0, 0);
      }
    }
  }
  barrier();
}

DI float red_g(float v) { v += __shfl_xor(v, 16); v += __shfl_xor(v, 32); return v; }

template <int MT>
DI void finish_stats(float (&s1)[MT], float (&s2)[MT], float (&cs)[3], float (&mu)[MT], float (&var)[MT], float* lcs, float* lst) {
  const int tid = threadIdx.x, lane = tid & 63, w = tid >> 6;
#pragma unroll
  for (int i = 0; i < MT; ++i) {
    float a = red_g(s1[i]) * (1.0f / D_), b = red_g(s2[i]) * (1.0f / D_);
    mu[i] = a; var[i] = fmaxf(b - a * a, 0.f);
    if (lane < 16) { int row = w * 16 * MT + 16 * i + lane; lst[row * 2] = mu[i]; lst[row * 2 + 1] = var[i]; }
  }
#pragma unroll
  for (int t = 0; t < 3; ++t) { float c = red_g(cs[t]); int j = w + 4 * t; if (j < 9 && lane < 16) lcs[j * 16 + lane] = c; }
}

__global__ __launch_bounds__(256) void k1_qkv(const bf16* __restrict__ x, const bf16* __restrict__ wqkv, const bf16* __restrict__ w1, char* __restrict__ ws) {
  extern __shared__ __attribute__((aligned(16))) char smem[];
  const int bid = blockIdx.x, xg = bid & 7, idx = bid >> 3;
  const int rt = (xg & 1) * 8 + (idx & 7), ct = (xg >> 1) * 4 + (idx >> 3);
  const int kind = ct >> 2;
  const int tid = threadIdx.x, lane = tid & 63, w = tid >> 6, r16 = lane & 15, q4 = lane >> 4;
  const bf16* W = kind < 3 ? wqkv : w1; const int ldw = kind < 3 ? 3 * D_ : D_; const int n0 = kind < 3 ? ct * 144 : (ct - 12) * 144;
  const bf16* xb = x + (size_t)rt * 128 * D_;
  auto xsrc = [&](int row, int k) { return xb + row * D_ + k; };
  f32x4 acc[2][9]; float s1[2], s2[2], cs[3], mu[2], var[2];
  if (kind == 2) gemm_core<128, false, true>(acc, s1, s2, cs, xsrc, W, ldw, n0, 0, 9, smem);
  else gemm_core<128, true, true>(acc, s1, s2, cs, xsrc, W, ldw, n0, 0, 9, smem);
  float* lcs = (float*)(smem + 40960); float* lst = (float*)(smem + 40960 + 1024);
  finish_stats<2>(s1, s2, cs, mu, var, lcs, lst);
  __syncthreads();
  if (ct == 0 && tid < 128) { float m = lst[tid * 2], v = lst[tid * 2 + 1];
    ((float*)(ws + WS_STAT))[rt * 128 + tid] = m; ((float*)(ws + WS_STAT))[S_ + rt * 128 + tid] = __builtin_amdgcn_rsqf(v + LN_EPS); }
  char* img = smem;
  if (kind <= 1) {
    const float sc = kind == 0 ? QSCALE : 1.0f;
#pragma unroll
    for (int i = 0; i < 2; ++i) {
      const float rs = __builtin_amdgcn_rsqf(var[i] + LN_EPS) * sc, m = mu[i];
      const int row = w * 32 + 16 * i + r16, kt = row >> 5, kr = row & 31;
#pragma unroll
      for (int j = 0; j < 9; ++j) {
        f32x4 c = *(const f32x4*)(lcs + 16 * j + 4 * q4);
#pragma unroll
        for (int e = 0; e < 2; ++e) {
          int n = 16 * j + 4 * q4 + 2 * e, hl = n / 18, d = n - hl * 18;
          unsigned v = pack2((acc[i][j][2 * e] - m * c[2 * e]) * rs, (acc[i][j][2 * e + 1] - m * c[2 * e + 1]) * rs);
          int off = hl * 4608 + (d < 16 ? kt * 1024 + (d >> 3) * 512 + kr * 16 + (d & 7) * 2 : 4096 + kt * 128 + kr * 4);
          *(unsigned*)(img + off) = v;
        }
      }
    }
    __syncthreads();
    if (kind == 1) {
      int hl = tid >> 5; float mx = 0.f;
#pragma unroll
      for (int i = 0; i < 4; ++i) {
        int kr = tid & 31; const char* b = img + hl * 4608;
        u32x4 a0 = *(const u32x4*)(b + i * 1024 + kr * 16), a1 = *(const u32x4*)(b + i * 1024 + 512 + kr * 16); unsigned a2 = *(const unsigned*)(b + 4096 + i * 128 + kr * 4);
        float s = 0.f;
#pragma unroll
        for (int e = 0; e < 4; ++e) { bf16x2 p = __builtin_bit_cast(bf16x2, a0[e]); s = __builtin_amdgcn_fdot2_f32_bf16(p, p, s, false); p = __builtin_bit_cast(bf16x2, a1[e]); s = __builtin_amdgcn_fdot2_f32_bf16(p, p, s, false); }
        { bf16x2 p = __builtin_bit_cast(bf16x2, a2); s = __builtin_amdgcn_fdot2_f32_bf16(p, p, s, false); }
        mx = fmaxf(mx, s);
      }
#pragma unroll
      for (int o = 1; o < 32; o <<= 1) mx = fmaxf(mx, __shfl_xor(mx, o));
      if ((tid & 31) == 0) ((float*)(ws + WS_KMAX))[((ct - 4) * 8 + hl) * 16 + rt] = mx;
    }
#pragma unroll
    for (int i = 0; i < 9; ++i) {
      int u = tid + 256 * i, hl = u / 288, o = (u - hl * 288) * 16;
      u32x4 v = *(const u32x4*)(img + u * 16);
      int head = (ct & 3) * 8 + hl; char* dst;
      if (kind == 0) dst = ws + WS_Q + ((size_t)head * 16 + rt) * QRT_B + o;
      else { char* bb = ws + WS_KV + (size_t)head * KVHEAD_B + (rt >> 1) * KVBLK_B; dst = o < 4096 ? bb + (rt & 1) * 4096 + o : bb + 8192 + (rt & 1) * 512 + (o - 4096); }
      *(u32x4*)dst = v;
    }
  } else if (kind == 2) {
#pragma unroll
    for (int i = 0; i < 2; ++i) {
      const int row0 = w * 32 + 16 * i + 4 * q4, step = row0 >> 4;
      float m[4], rs[4];
#pragma unroll
      for (int e = 0; e < 4; ++e) { m[e] = lst[(row0 + e) * 2]; rs[e] = __builtin_amdgcn_rsqf(lst[(row0 + e) * 2 + 1] + LN_EPS); }
#pragma unroll
      for (int j = 0; j < 9; ++j) {
        int n = 16 * j + r16, hl = n / 18, d = n - hl * 18; float c = lcs[n];
        u32x2 v = {pack2((acc[i][j][0] - m[0] * c) * rs[0], (acc[i][j][1] - m[1] * c) * rs[1]), pack2((acc[i][j][2] - m[2] * c) * rs[2], (acc[i][j][3] - m[3] * c) * rs[3])};
        *(u32x2*)(img + hl * 4864 + step * 608 + (q4 & 1) * 304 + d * 16 + (q4 >> 1) * 8) = v;
      }
    }
    if (tid < 128) { int hl = tid >> 4, sh = tid & 15; u32x4 ones = {0x3f803f80u, 0x3f803f80u, 0x3f803f80u, 0x3f803f80u}; *(u32x4*)(img + hl * 4864 + sh * 304 + 18 * 16) = ones; }
    __syncthreads();
    for (int u = tid; u < 8 * 304; u += 256) {
      int hl = u / 304, o = (u - hl * 304) * 16; int head = (ct & 3) * 8 + hl;
      u32x4 v = *(const u32x4*)(img + u * 16);
      *(u32x4*)(ws + WS_KV + (size_t)head * KVHEAD_B + (rt >> 1) * KVBLK_B + 9216 + (rt & 1) * 4864 + o) = v;
    }
  } else {
#pragma unroll
    for (int i = 0; i < 2; ++i) {
      const float r1 = __builtin_amdgcn_rsqf(var[i] + LN_EPS), v2 = var[i] * r1 * r1, rs = r1 * __builtin_amdgcn_rsqf(v2 + LN_EPS), m = mu[i];
      const int row = w * 32 + 16 * i + r16;
#pragma unroll
      for (int j = 0; j < 9; ++j) {
        f32x4 c = *(const f32x4*)(lcs + 16 * j + 4 * q4);
        u32x2 v = {pack2(gelu_tanh((acc[i][j][0] - m * c[0]) * rs), gelu_tanh((acc[i][j][1] - m * c[1]) * rs)),
                   pack2(gelu_tanh((acc[i][j][2] - m * c[2]) * rs), gelu_tanh((acc[i][j][3] - m * c[3]) * rs))};
        *(u32x2*)(img + row * 304 + (16 * j + 4 * q4) * 2) = v;
      }
    }
    __syncthreads();
    char* hb = ws + WS_H + (size_t)(2 + rt * 128) * D_ * 2 + (ct - 12) * 288;
#pragma unroll
    for (int i = 0; i < 9; ++i) { int u = tid + 256 * i, row = u / 18, c = u - row * 18; *(u32x4*)(hb + (size_t)row * D_ * 2 + c * 16) = *(const u32x4*)(img + row * 304 + c * 16); }
    if (rt == 0 || rt == 15) {
      if (tid < 36) { int row = tid / 18, c = tid - row * 18; u32x4 z = {0, 0, 0, 0};
        *(u32x4*)(ws + WS_H + (size_t)((rt == 0 ? 0 : S_ + 2) + row) * D_ * 2 + (ct - 12) * 288 + c * 16) = z; }
    }
  }
}

__global__ __launch_bounds__(256) void k2_w2(const bf16* __restrict__ x, const bf16* __restrict__ w2, char* __restrict__ ws) {
  extern __shared__ __attribute__((aligned(16))) char smem[];
  const int bid = blockIdx.x, xg = bid & 7, idx = bid >> 3;
  const int rt = xg * 8 + (idx & 7), ct = idx >> 3;
  const int tid = threadIdx.x, lane = tid & 63, w = tid >> 6, r16 = lane & 15, q4 = lane >> 4;
  const bf16* hb = (const bf16*)(ws + WS_H) + (size_t)(2 + rt * 32) * D_;
  auto xsrc = [&](int row, int k) { int sh = k < 144 ? 1 : (k < 288 ? -1 : (k < 432 ? 2 : -2)); return hb + (row - sh) * D_ + k; };
  f32x4 acc[1][5]; float s1[1], s2[1], cs[3];
  gemm_core<32, true, false>(acc, s1, s2, cs, xsrc, w2, D_, ct * 144, 0, 9, smem);
  const int row = rt * 32 + (w & 1) * 16 + r16, jt0 = (w >> 1) * 4;
  const float mu = ((const float*)(ws + WS_STAT))[row], rs = ((const float*)(ws + WS_STAT))[S_ + row];
#pragma unroll
  for (int j = 0; j < 5; ++j) {
    int n = ct * 144 + (jt0 + j) * 16 + 4 * q4;
    u32x2 xv = *(const u32x2*)(x + (size_t)row * D_ + n);
    u32x2 v = {pack2((bf_lo(xv[0]) - mu) * rs + acc[0][j][0], (bf_hi(xv[0]) - mu) * rs + acc[0][j][1]), pack2((bf_lo(xv[1]) - mu) * rs + acc[0][j][2], (bf_hi(xv[1]) - mu) * rs + acc[0][j][3])};
    *(u32x2*)((bf16*)(ws + WS_X1) + (size_t)row * D_ + n) = v;
  }
}

constexpr int ATT_LDS = KVHEAD_B;
__global__ __launch_bounds__(512) void k3_attn(char* __restrict__ ws) {
  extern __shared__ __attribute__((aligned(16))) char smem[];
  const int bid = blockIdx.x, head = (bid & 7) * 4 + ((bid >> 3) & 3), qb = bid >> 5;
  const int tid = threadIdx.x, lane = tid & 63, w = __builtin_amdgcn_readfirstlane(tid >> 6), r = lane & 31, h = lane >> 5;
  const char* kv = ws + WS_KV + (size_t)head * KVHEAD_B;
#pragma unroll
  for (int st = 0; st < 4; ++st)
#pragma unroll
    for (int i = 0; i < 5; ++i) { int p = w + 8 * i; p = p > 36 ? 36 : p; p += st * 37; dma16(kv + p * 1024 + lane * 16, smem + p * 1024); }
  const int qt = qb * 8 + w;
  const char* qp = ws + WS_Q + ((size_t)head * 16 + (qt >> 2)) * QRT_B;
  u32x4 q0u = *(const u32x4*)(qp + (qt & 3) * 1024 + lane * 16);
  unsigned qr = *(const unsigned*)(qp + 4096 + (qt & 3) * 128 + r * 4);
  float km = 0.f;
  { const float* kp = (const float*)(ws + WS_KMAX) + head * 16;
#pragma unroll
    for (int i = 0; i < 16; ++i) km = fmaxf(km, kp[i]); }
  float qs = 0.f;
#pragma unroll
  for (int e = 0; e < 4; ++e) { bf16x2 p = __builtin_bit_cast(bf16x2, q0u[e]); qs = __builtin_amdgcn_fdot2_f32_bf16(p, p, qs, false); }
  if (h == 0) { bf16x2 p = __builtin_bit_cast(bf16x2, qr); qs = __builtin_amdgcn_fdot2_f32_bf16(p, p, qs, false); }
  qs += __shfl_xor(qs, 32);
  const float mb = __builtin_sqrtf(qs * km) * 1.001f;
  const bool fast = __all(mb <= 45.0f);
  const bf16x8 q0 = __builtin_bit_cast(bf16x8, q0u);
  u32x4 q1u = {0, 0, 0, 0};
  if (h == 0) { q1u[0] = qr; q1u[1] = fast ? (pack2(-mb, 0.f) & 0xffffu) : 0u; }
  const bf16x8 q1 = __builtin_bit_cast(bf16x8, q1u);
  const int vl = 9216 + h * 304 + (r < 19 ? r : (r >= 20 ? r - 16 : 3)) * 16;
  f32x16 O;
#pragma unroll
  for (int i = 0; i < 16; ++i) O[i] = 0.f;
  const f32x16 zero = O;
  float mrun = -3.0e38f;
  for (int st = 0; st < 4; ++st) {
    if (st == 0) wait_vm<15>(); else if (st == 1) wait_vm<10>(); else if (st == 2) wait_vm<5>(); else wait_vm<0>();
    barrier();
    if (fast) {
#pragma unroll 2
      for (int t = 0; t < 16; ++t) {
        const char* bb = smem + (st * 2 + (t >> 3)) * KVBLK_B; const int kt = t & 7;
        bf16x8 kf = *(const bf16x8*)(bb + kt * 1024 + lane * 16);
        u32x4 k1 = {*(const unsigned*)(bb + 8192 + kt * 128 + r * 4), 0x3f80u, 0u, 0u};
        bf16x8 v0 = *(const bf16x8*)(bb + vl + kt * 1216), v1 = *(const bf16x8*)(bb + vl + kt * 1216 + 608);
        f32x16 s = __builtin_amdgcn_mfma_f32_32x32x16_bf16(kf, q0, zero, 0, 0, 0);
        s = __builtin_amdgcn_mfma_f32_32x32x16_bf16(__builtin_bit_cast(bf16x8, k1), q1, s, 0, 0, 0);
        u32x4 p0, p1;
#pragma unroll
        for (int e = 0; e < 4; ++e) { p0[e] = pack2(__builtin_amdgcn_exp2f(s[2 * e]), __builtin_amdgcn_exp2f(s[2 * e + 1])); p1[e] = pack2(__builtin_amdgcn_exp2f(s[8 + 2 * e]), __builtin_amdgcn_exp2f(s[9 + 2 * e])); }
        O = __builtin_amdgcn_mfma_f32_32x32x16_bf16(v0, __builtin_bit_cast(bf16x8, p0), O, 0, 0, 0);
        O = __builtin_amdgcn_mfma_f32_32x32x16_bf16(v1, __builtin_bit_cast(bf16x8, p1), O, 0, 0, 0);
      }
    } else {
      for (int t = 0; t < 16; ++t) {
        const char* bb = smem + (st * 2 + (t >> 3)) * KVBLK_B; const int kt = t & 7;
        bf16x8 kf = *(const bf16x8*)(bb + kt * 1024 + lane * 16);
        u32x4 k1 = {*(const unsigned*)(bb + 8192 + kt * 128 + r * 4), 0x3f80u, 0u, 0u};
        bf16x8 v0 = *(const bf16x8*)(bb + vl + kt * 1216), v1 = *(const bf16x8*)(bb + vl + kt * 1216 + 608);
        f32x16 s = __builtin_amdgcn_mfma_f32_32x32x16_bf16(kf, q0, zero, 0, 0, 0);
        s = __builtin_amdgcn_mfma_f32_32x32x16_bf16(__builtin_bit_cast(bf16x8, k1), q1, s, 0, 0, 0);
        float mx = s[0];
#pragma unroll
        for (int e = 1; e < 16; ++e) mx = fmaxf(mx, s[e]);
        mx = fmaxf(mx, __shfl_xor(mx, 32));
        const float mn = fmaxf(mrun, mx), al = __builtin_amdgcn_exp2f(mrun - mn); mrun = mn;
#pragma unroll
        for (int e = 0; e < 16; ++e) O[e] *= al;
        u32x4 p0, p1;
#pragma unroll
        for (int e = 0; e < 4; ++e) { p0[e] = pack2(__builtin_amdgcn_exp2f(s[2 * e] - mn), __builtin_amdgcn_exp2f(s[2 * e + 1] - mn)); p1[e] = pack2(__builtin_amdgcn_exp2f(s[8 + 2 * e] - mn), __builtin_amdgcn_exp2f(s[9 + 2 * e] - mn)); }
        O = __builtin_amdgcn_mfma_f32_32x32x16_bf16(v0, __builtin_bit_cast(bf16x8, p0), O, 0, 0, 0);
        O = __builtin_amdgcn_mfma_f32_32x32x16_bf16(v1, __builtin_bit_cast(bf16x8, p1), O, 0, 0, 0);
      }
    }
  }
  const float l = __shfl(O[10], r), il = 1.0f / l;
  char* op = ws + WS_O + (size_t)(qt * 32 + r) * D_ * 2 + head * 36 + h * 8;
  *(u32x2_a4*)(op) = u32x2{pack2(O[0] * il, O[1] * il), pack2(O[2] * il, O[3] * il)};
  *(u32x2_a4*)(op + 16) = u32x2{pack2(O[4] * il, O[5] * il), pack2(O[6] * il, O[7] * il)};
  if (h == 0) *(unsigned*)(op + 32) = pack2(O[8] * il, O[9] * il);
}

__global__ __launch_bounds__(256) void k4a_w3(const bf16* __restrict__ w3, char* __restrict__ ws) {
  extern __shared__ __attribute__((aligned(16))) char smem[];
  const int bid = blockIdx.x, xg = bid & 7, idx = bid >> 3;
  const int rt = (idx & 15), ct = xg * 4 + (idx >> 4);
  const int tid = threadIdx.x, lane = tid & 63, w = tid >> 6, r16 = lane & 15, q4 = lane >> 4;
  const bf16* xb = (const bf16*)(ws + WS_X1) + (size_t)rt * 128 * D_;
  auto xsrc = [&](int row, int k) { return xb + row * D_ + k; };
  f32x4 acc[2][9]; float s1[2], s2[2], cs[3], mu[2], var[2];
  gemm_core<128, true, true>(acc, s1, s2, cs, xsrc, w3, F_, ct * 144, 0, 9, smem);
  float* lcs = (float*)(smem + 40960); float* lst = (float*)(smem + 40960 + 1024);
  finish_stats<2>(s1, s2, cs, mu, var, lcs, lst);
  __syncthreads();
  char* img = smem;
#pragma unroll
  for (int i = 0; i < 2; ++i) {
    const float rs = __builtin_amdgcn_rsqf(var[i] + LN_EPS), m = mu[i];
    const int row = w * 32 + 16 * i + r16;
#pragma unroll
    for (int j = 0; j < 9; ++j) {
      f32x4 c = *(const f32x4*)(lcs + 16 * j + 4 * q4);
      u32x2 v = {pack2(gelu_tanh((acc[i][j][0] - m * c[0]) * rs), gelu_tanh((acc[i][j][1] - m * c[1]) * rs)),
                 pack2(gelu_tanh((acc[i][j][2] - m * c[2]) * rs), gelu_tanh((acc[i][j][3] - m * c[3]) * rs))};
      *(u32x2*)(img + row * 304 + (16 * j + 4 * q4) * 2) = v;
    }
  }
  __syncthreads();
  char* hb = ws + WS_HH + (size_t)(rt * 128) * F_ * 2 + ct * 288;
#pragma unroll
  for (int i = 0; i < 9; ++i) { int u = tid + 256 * i, row = u / 18, c = u - row * 18; *(u32x4*)(hb + (size_t)row * F_ * 2 + c * 16) = *(const u32x4*)(img + row * 304 + c * 16); }
}

__global__ __launch_bounds__(256) void k4b_w4(const bf16* __restrict__ w4, char* __restrict__ ws) {
  extern __shared__ __attribute__((aligned(16))) char smem[];
  const int bid = blockIdx.x, xg = bid & 7, idx = bid >> 3;
  const int rt = (xg & 1) * 8 + (idx & 7), sp = xg >> 1, ct = idx >> 3;
  const int tid = threadIdx.x, lane = tid & 63, w = tid >> 6, r16 = lane & 15, q4 = lane >> 4;
  const bf16* xb = (const bf16*)(ws + WS_HH) + (size_t)rt * 128 * F_;
  auto xsrc = [&](int row, int k) { return xb + (size_t)row * F_ + k; };
  f32x4 acc[2][9]; float s1[2], s2[2], cs[3];
  gemm_core<128, true, false>(acc, s1, s2, cs, xsrc, w4, D_, ct * 144, sp * 1152, 18, smem);
  float* pb = (float*)(ws + WS_PART) + (size_t)sp * S_ * D_;
#pragma unroll
  for (int i = 0; i < 2; ++i) {
    const int row = rt * 128 + w * 32 + 16 * i + r16;
#pragma unroll
    for (int j = 0; j < 9; ++j) *(f32x4*)(pb + (size_t)row * D_ + ct * 144 + 16 * j + 4 * q4) = acc[i][j];
  }
}

__global__ __launch_bounds__(256) void k5_final(const bf16* __restrict__ x, const bf16* __restrict__ wo, char* __restrict__ ws, bf16* __restrict__ out) {
  extern __shared__ __attribute__((aligned(16))) char smem[];
  const int bid = blockIdx.x, xg = bid & 7, idx = bid >> 3;
  const int rt = xg * 8 + (idx & 7), ct = idx >> 3;
  const int tid = threadIdx.x, lane = tid & 63, w = tid >> 6, r16 = lane & 15, q4 = lane >> 4;
  const bf16* ob = (const bf16*)(ws + WS_O) + (size_t)(rt * 32) * D_;
  auto xsrc = [&](int row, int k) { return ob + row * D_ + k; };
  f32x4 acc[1][5]; float s1[1], s2[1], cs[3];
  gemm_core<32, true, false>(acc, s1, s2, cs, xsrc, wo, D_, ct * 144, 0, 9, smem);
  const int row = rt * 32 + (w & 1) * 16 + r16, jt0 = (w >> 1) * 4;
#pragma unroll
  for (int j = 0; j < 5; ++j) {
    int n = ct * 144 + (jt0 + j) * 16 + 4 * q4; size_t o = (size_t)row * D_ + n;
    u32x2 xv = *(const u32x2*)(x + o), x1v = *(const u32x2*)((const bf16*)(ws + WS_X1) + o);
    f32x4 p = {bf_lo(x1v[0]), bf_hi(x1v[0]), bf_lo(x1v[1]), bf_hi(x1v[1])};
#pragma unroll
    for (int sp = 0; sp < 4; ++sp) p += *(const f32x4*)((const float*)(ws + WS_PART) + (size_t)sp * S_ * D_ + o);
    float xs[4] = {bf_lo(xv[0]), bf_hi(xv[0]), bf_lo(xv[1]), bf_hi(xv[1])}, r[4];
#pragma unroll
    for (int e = 0; e < 4; ++e) { float g = p[e] * __builtin_amdgcn_rcpf(1.0f + __builtin_amdgcn_exp2f(-1.4426950408889634f * p[e])); r[e] = xs[e] + acc[0][j][e] * g; }
    *(u32x2*)(out + o) = u32x2{pack2(r[0], r[1]), pack2(r[2], r[3])};
  }
}

extern "C" void kernel_launch(void* const* d_in, const int* in_sizes, int n_in, void* d_out, int out_size, void* d_ws, size_t ws_size, hipStream_t stream) {
  const bf16* x = (const bf16*)d_in[0]; const bf16* wqkv = (const bf16*)d_in[1]; const bf16* wo = (const bf16*)d_in[2];
  const bf16* w1 = (const bf16*)d_in[4]; const bf16* w2 = (const bf16*)d_in[5]; const bf16* w3 = (const bf16*)d_in[6]; const bf16* w4 = (const bf16*)d_in[7];
  char* ws = (char*)d_ws;
  constexpr int L128 = G<128>::STAGE * 4, L32 = G<32>::STAGE * 4;
  (void)hipFuncSetAttribute((const void*)k1_qkv, hipFuncAttributeMaxDynamicSharedMemorySize, L128);
  (void)hipFuncSetAttribute((const void*)k2_w2, hipFuncAttributeMaxDynamicSharedMemorySize, L32);
  (void)hipFuncSetAttribute((const void*)k3_attn, hipFuncAttributeMaxDynamicSharedMemorySize, ATT_LDS);
  (void)hipFuncSetAttribute((const void*)k4a_w3, hipFuncAttributeMaxDynamicSharedMemorySize, L128);
  (void)hipFuncSetAttribute((const void*)k4b_w4, hipFuncAttributeMaxDynamicSharedMemorySize, L128);
  (void)hipFuncSetAttribute((const void*)k5_final, hipFuncAttributeMaxDynamicSharedMemorySize, L32);
  k1_qkv<<<256, 256, L128, stream>>>(x, wqkv, w1, ws);
  k2_w2<<<256, 256, L32, stream>>>(x, w2, ws);
  k3_attn<<<256, 512, ATT_LDS, stream>>>(ws);
  k4a_w3<<<512, 256, L128, stream>>>(w3, ws);
  k4b_w4<<<256, 256, L128, stream>>>(w4, ws);
  k5_final<<<256, 256, L32, stream>>>(x, wo, ws, (bf16*)d_out);
}
```

```cpp
#include <hip/hip_runtime.h>
#include <hip/hip_bf16.h>
#include <stdint.h>

typedef __bf16 bf16;
typedef __bf16 bf16x2 __attribute__((ext_vector_type(2)));
typedef __bf16 bf16x4 __attribute__((ext_vector_type(4)));
typedef __bf16 bf16x8 __attribute__((ext_vector_type(8)));
typedef short s16x4 __attribute__((ext_vector_type(4)));
typedef float f32x2 __attribute__((ext_vector_type(2)));
typedef float f32x4 __attribute__((ext_vector_type(4)));
typedef float f32x16 __attribute__((ext_vector_type(16)));
typedef unsigned u32x2 __attribute__((ext_vector_type(2)));
typedef unsigned u32x4 __attribute__((ext_vector_type(4)));
typedef u32x2 u32x2_a4 __attribute__((aligned(4)));
#define LDS_AS __attribute__((address_space(3)))
#define GLB_AS __attribute__((address_space(1)))
#define DI __device__ __forceinline__

constexpr int S_ = 2048, D_ = 576, F_ = 4608, NH = 32, DH = 18;
constexpr float LN_EPS = 1e-5f;
constexpr float QSCALE = 0.23570226039551584f * 1.4426950408889634f;

constexpr size_t WS_STAT = 0;
constexpr size_t WS_KMAX = WS_STAT + 2 * S_ * 4;
constexpr size_t WS_Q    = WS_KMAX + 32 * 16 * 4;
constexpr int    QRT_B   = 4608;
constexpr int    KVBLK_B = 18944;
constexpr int    KVHEAD_B = 8 * KVBLK_B;
constexpr size_t WS_KV   = WS_Q + (size_t)NH * 16 * QRT_B;
constexpr size_t WS_H    = WS_KV + (size_t)NH * KVHEAD_B;
constexpr size_t WS_X1   = WS_H + (size_t)(S_ + 4) * D_ * 2;
constexpr size_t WS_O    = WS_X1 + (size_t)S_ * D_ * 2;
constexpr size_t WS_ST1  = WS_O + (size_t)S_ * D_ * 2;
constexpr int    NSPLIT  = 8;
constexpr size_t WS_PART = WS_ST1 + (size_t)S_ * 128;
constexpr size_t WS_S    = WS_PART + (size_t)NSPLIT * S_ * D_ * 2;
constexpr size_t WS_CNT  = WS_S + (size_t)S_ * D_ * 2;
constexpr size_t WS_END  = WS_CNT + 32 * 128;

DI unsigned pack2(float lo, float hi) { f32x2 v = {lo, hi}; bf16x2 b = __builtin_convertvector(v, bf16x2); return __builtin_bit_cast(unsigned, b); }
DI bf16x2 as_bf2(unsigned u) { return __builtin_bit_cast(bf16x2, u); }
DI float bf_lo(unsigned u) { return __builtin_bit_cast(float, u << 16); }
DI float bf_hi(unsigned u) { return __builtin_bit_cast(float, u & 0xffff0000u); }
DI float gelu_tanh(float x) {
  const float c0 = -2.0f * 0.7978845608028654f * 1.4426950408889634f;
  const float c1 = c0 * 0.044715f;
  float t = x * x;
  float a = x * __builtin_fmaf(t, c1, c0);
  float e = __builtin_amdgcn_exp2f(a);
  return x * __builtin_amdgcn_rcpf(1.0f + e);
}
#define STAMP_SEL -1
#define STAMPK(id, i) do { if (STAMP_SEL == (id)) { if ((threadIdx.x & 63) == 0) { ((unsigned long long*)(g_ws_dbg + (200u << 20)))[(blockIdx.x * 16 + (threadIdx.x >> 6)) * 8 + (i)] = __builtin_amdgcn_s_memtime(); } } } while (0)
DI void dma16(const void* g, char* lds) {
  __builtin_amdgcn_global_load_lds((const GLB_AS void*)g, (LDS_AS void*)lds, 16, 0, 0);
}
template <int N> DI void wait_vm() { asm volatile("s_waitcnt vmcnt(%0)" ::"n"(N) : "memory"); }
DI void barrier() { asm volatile("s_waitcnt lgkmcnt(0)\n\ts_barrier" ::: "memory"); }

#define CODE_ADDR(p, sym) asm volatile("s_getpc_b64 s[96:97]\n\tLPC_%=:\n\ts_add_u32 s96, s96, " sym "-LPC_%=\n\ts_addc_u32 s97, s97, (" sym "-LPC_%=)>>32\n\ts_mov_b64 %0, s[96:97]" : "=s"(p) :: "s96", "s97", "scc")
#define SYM_K1 "_Z6k1_qkvPKDF16bS0_S0_S0_S0_S0_S0_Pc"
#define SYM_K34 "_Z12k34_attn_mlpPKDF16bS0_S0_S0_S0_Pc"
#define SYM_K5 "_Z8k5_finalPKDF16bPKcPDF16b"
#define SYM_ADDR(p, sym) asm volatile("s_getpc_b64 s[96:97]\n\ts_add_u32 s96, s96, " sym "@rel32@lo+4\n\ts_addc_u32 s97, s97, " sym "@rel32@hi+12\n\ts_mov_b64 %0, s[96:97]" : "=s"(p) :: "s96", "s97", "scc")
#define CODE_LINES(n, symA, symB) do { int nb_; asm volatile("s_mov_b32 %0, " symB "-" symA : "=s"(nb_)); n = nb_ > 0 ? (nb_ >> 7 < 640 ? nb_ >> 7 : 640) : 0; } while (0)
template <int BM_, int WM_, int NCG_, int NH_, int NS_, int PD_, int AH_, int NSW_ = 0, int NSX_ = 0> struct G {
  static constexpr int AH = AH_;
  static constexpr int BM = BM_, WM = WM_, NCG = NCG_, NH = NH_, NS = NS_, NC = WM * NCG, NSW = NSW_, NW = NC + NH + NSW;
  static constexpr int PD = PD_;
  static constexpr int LAG = PD_ == 1 ? 1 : 2;
  static constexpr int WP = 320;
  static constexpr int XB = BM * 128, WB = 64 * WP, STAGE = XB + WB;
  static constexpr int XP = BM / 8, P = XP + 20;
  static constexpr int xs(int s) { return NSX_ ? (s % (NSX_ ? NSX_ : 1)) * XB : (s % NS) * STAGE; }
  static constexpr int wsl(int s) { return NSX_ ? NSX_ * XB + (s % NS) * WB - XB : (s % NS) * STAGE; }
  static constexpr int L = P / (NH ? NH : NC);
  static_assert(NSX_ || AH_ <= NS_ - (PD_ == 1 ? 1 : 2), "ring too short");
  static constexpr int SPARE = 3328;
  static_assert(P % (NH ? NH : NC) == 0, "pieces divide evenly");
  static constexpr int MT = BM / 16 / WM;
  static constexpr int NTW = NCG == 3 ? 3 : 5, JS = NCG == 3 ? 3 : 4;
};
#define SBAR() __builtin_amdgcn_sched_barrier(0)
DI float u2f(unsigned u) { return __builtin_bit_cast(float, u); }
DI unsigned f2u(float f) { return __builtin_bit_cast(unsigned, f); }
DI float add_x16(float v) { auto r = __builtin_amdgcn_permlane16_swap(f2u(v), f2u(v), false, false); const unsigned a = r[0], b = r[1]; return u2f(a) + u2f(b); }
DI float add_x32(float v) { auto r = __builtin_amdgcn_permlane32_swap(f2u(v), f2u(v), false, false); const unsigned a = r[0], b = r[1]; return u2f(a) + u2f(b); }
DI float add_x1(float v) { return v + u2f((unsigned)__builtin_amdgcn_update_dpp(0, (int)f2u(v), 0xB1, 0xf, 0xf, false)); }
DI float red_g(float v) { return add_x32(add_x16(v)); }
template <int N> DI void wait_vm_c() { wait_vm<(N > 63 ? 63 : N)>(); }

DI void wait_vm_rt(int n) {
  if (n >= 32) wait_vm<32>(); else if (n >= 24) wait_vm<24>(); else if (n >= 18) wait_vm<18>(); else if (n >= 15) wait_vm<15>(); else if (n >= 12) wait_vm<12>();
  else if (n >= 10) wait_vm<10>(); else if (n >= 9) wait_vm<9>(); else if (n >= 8) wait_vm<8>(); else if (n >= 6) wait_vm<6>(); else if (n >= 4) wait_vm<4>();
  else if (n >= 3) wait_vm<3>(); else if (n >= 2) wait_vm<2>(); else if (n >= 1) wait_vm<1>(); else wait_vm<0>();
}
struct Src { const char* xb; unsigned xbytes, xoff; const char* wb; unsigned wbytes, woff, ldw; int rot; };
template <typename g, int N, int MODE> struct Pieces {
  static constexpr bool SHIFT = MODE == 1;
  static constexpr int STEP = g::P / N, NX = g::XP / STEP;
  static_assert(g::P % N == 0 && g::XP % STEP == 0, "piece split");
  __amdgpu_buffer_rsrc_t rx, rw; int voff[N], u8[NX], first, ws64, rot;
  DI void init(const Src& sc, int first_, int lane) {
    first = first_; ws64 = 64 * (int)sc.ldw; rot = sc.rot;
    rx = __builtin_amdgcn_make_buffer_rsrc((void*)sc.xb, 0, sc.xbytes, 0x00020000); rw = __builtin_amdgcn_make_buffer_rsrc((void*)sc.wb, 0, sc.wbytes, 0x00020000);
#pragma unroll
    for (int i = 0; i < N; ++i) {
      const int p = first + STEP * i;
      if (i < NX) { const int row = p * 8 + (lane >> 3), u = (lane & 7) ^ ((row >> 1) & 7); voff[i] = (int)sc.xoff + row * 1152 + u * 16; u8[i < NX ? i : 0] = u * 8; }
      else {
        const int q = (p - g::XP) * 64 + lane, rho = q / 20, k = (rho & ~12) | ((rho & 4) << 1) | ((rho & 8) >> 1);
        voff[i] = (int)sc.woff + k * (int)sc.ldw + ((q - rho * 20) ^ ((rho >> 1) & 2)) * 16;
      }
    }
  }
  DI void issue(int s, char* smem, int part = 3) const {
    char* st = smem + g::xs(s) + first * 1024; char* sw = smem + g::wsl(s) + first * 1024;
    int c = s + rot; c = c >= 9 ? c - 9 : c;
#pragma unroll
    for (int i = 0; i < N; ++i) {
      if (i < NX ? !(part & 1) : !(part & 2)) continue;
      if (i < NX) {
        int v = voff[i];
        if (SHIFT) { const int k = c * 64 + u8[i < NX ? i : 0], sh = k < 144 ? 1 : (k < 288 ? -1 : (k < 432 ? 2 : -2)); v -= sh * 1152; }
        __builtin_amdgcn_raw_ptr_buffer_load_lds(rx, (LDS_AS void*)(st + STEP * i * 1024), 16, v, c * 128, 0, 0);
      } else __builtin_amdgcn_raw_ptr_buffer_load_lds(rw, (LDS_AS void*)(sw + STEP * i * 1024), 16, voff[i], c * ws64, 0, 0);
    }
  }
};
template <typename g, int SID, int NT, int SHIFT, typename PADDR>
DI void gemm_helper(char* g_ws_dbg, const Src& sc, int hw, int lane, char* smem, PADDR paddr, unsigned& sink) {
  Pieces<g, g::L, SHIFT> pc; pc.init(sc, hw, lane);
#pragma unroll
  for (int s = 0; s < g::AH; ++s) pc.issue(s, smem);
  constexpr int SLAST = 8 - g::AH;
#pragma unroll 1
  for (int s = 0; s < 9; ++s) {
    int last = s + g::AH - 1; last = last < 8 ? last : 8;
    wait_vm_rt((last - s) * g::L + (s > SLAST ? NT : 0));
    barrier();
    if (s == 0) STAMPK(SID, 1);
    if (s + g::AH < 9) pc.issue(s + g::AH, smem);
    if (s == SLAST) {
#pragma unroll
      for (int i = 0; i < NT; ++i) { const void* a = paddr(i); asm volatile("global_load_dword %0, %1, off" : "+v"(sink) : "v"(a) : "memory"); }
    }
  }
  STAMPK(SID, 2);
}

template <typename g>
DI void gemm_stats(int sw, int lane, char* smem, float* lst, float* lcs, int odd) {
  float s1a = 0.f, s1b = 0.f, s2a = 0.f, s2b = 0.f, cs[5] = {0.f, 0.f, 0.f, 0.f, 0.f};
  const int srow = sw * 32 + (lane >> 1), shalf = lane & 1;
  const int q4 = lane >> 4, r16 = lane & 15, jt0 = (sw >> 1) * 4;
  const int wrow = g::XB + (32 * (sw & 1) + 16 * (q4 >> 1) + 4 * (q4 & 1) + (r16 >> 2)) * g::WP + (lane & 3) * 8;
  const int wb0 = wrow + ((odd + jt0) ^ (q4 & 1)) * 32, wb1 = wrow + ((odd + jt0 + 1) ^ (q4 & 1)) * 32;
  int xa[4];
#pragma unroll
  for (int e = 0; e < 4; ++e) xa[e] = srow * 128 + (((4 * shalf + e) ^ ((srow >> 1) & 7)) << 4);
  const bf16x2 one = {(bf16)1.0f, (bf16)1.0f};
  struct Buf { u32x4 v[4]; u32x2 lo[5], hi[5]; };
  const unsigned sb = (unsigned)(size_t)(LDS_AS char*)smem;
  auto rd = [&](int s, Buf& B) {
    const unsigned st = sb + (s % g::NS) * g::STAGE;
#pragma unroll
    for (int e = 0; e < 4; ++e) asm volatile("ds_read_b128 %0, %1" : "=v"(B.v[e]) : "v"(st + xa[e]) : "memory");
#pragma unroll
    for (int j = 0; j < 5; ++j) {
      const unsigned a = st + ((j & 1) ? wb1 + (j - 1) * 32 : wb0 + j * 32);
      asm volatile("ds_read_b64_tr_b16 %0, %2\n\tds_read_b64_tr_b16 %1, %2 offset:%3" : "=&v"(B.lo[j]), "=&v"(B.hi[j]) : "v"(a), "i"(8 * g::WP) : "memory");
    }
  };
  auto tie = [&](Buf& B) {
    asm volatile("" : "+v"(B.v[0]), "+v"(B.v[1]), "+v"(B.v[2]), "+v"(B.v[3]), "+v"(B.lo[0]), "+v"(B.lo[1]), "+v"(B.lo[2]), "+v"(B.lo[3]), "+v"(B.lo[4]),
                      "+v"(B.hi[0]), "+v"(B.hi[1]), "+v"(B.hi[2]), "+v"(B.hi[3]), "+v"(B.hi[4]));
  };
  auto acc = [&](const Buf& B) {
#pragma unroll
    for (int e = 0; e < 4; ++e)
#pragma unroll
      for (int c = 0; c < 4; c += 2) {
        const unsigned u = B.v[e][c], u2 = B.v[e][c + 1];
        s1a = __builtin_amdgcn_fdot2_f32_bf16(as_bf2(u), one, s1a, false); s2a = __builtin_amdgcn_fdot2_f32_bf16(as_bf2(u), as_bf2(u), s2a, false);
        s1b = __builtin_amdgcn_fdot2_f32_bf16(as_bf2(u2), one, s1b, false); s2b = __builtin_amdgcn_fdot2_f32_bf16(as_bf2(u2), as_bf2(u2), s2b, false);
      }
#pragma unroll
    for (int c = 0; c < 2; ++c)
#pragma unroll
      for (int j = 0; j < 5; ++j) { const unsigned a = B.lo[j][c], b = B.hi[j][c]; cs[j] = __builtin_amdgcn_fdot2_f32_bf16(as_bf2(a), one, cs[j], false); cs[j] = __builtin_amdgcn_fdot2_f32_bf16(as_bf2(b), one, cs[j], false); }
  };
  Buf A, B;
  barrier(); rd(0, A);
#pragma unroll 1
  for (int s = 1; s < 9; s += 2) {
    barrier(); tie(A); rd(s, B); SBAR(); acc(A); SBAR();
    barrier(); tie(B); rd(s + 1, A); SBAR(); acc(B); SBAR();
  }
  asm volatile("s_waitcnt lgkmcnt(0)" ::: "memory"); tie(A);
  acc(A);
  const float s1 = add_x1(s1a + s1b) * (1.0f / D_), s2 = add_x1(s2a + s2b) * (1.0f / D_);
  { const float var = fmaxf(s2 - s1 * s1, 0.f), r1 = __builtin_amdgcn_rsqf(var + LN_EPS), r2 = r1 * __builtin_amdgcn_rsqf(var * r1 * r1 + LN_EPS);
    if (shalf == 0) *(f32x4*)(lst + srow * 4) = f32x4{s1, r1, r2, var}; }
#pragma unroll
  for (int j = 0; j < 5; ++j) { const float c = red_g(cs[j]); if (lane < 16 && (j > 0 || jt0 == 0)) lcs[(sw & 1) * 144 + (jt0 + j) * 16 + lane] = c; }
}

#define FST(i)
#define BST(i)
struct NoHook { DI void before(int) const {} DI void after(int) const {} };
template <typename g, bool TRANS, bool STATS, bool XLIN = false, typename HOOK = NoHook>
DI void gemm_consumer(f32x4 (&acc)[g::MT][g::NTW], int w, int lane, char* smem, float* lst, float* lcs, int odd, HOOK hook = HOOK()) {
  const int r16 = lane & 15, q4 = lane >> 4;
  const int wm = w % g::WM, wn = w / g::WM;
  const int wrow0 = wm * 16 * g::MT, jt0 = wn * g::JS;
  const int xa0 = XLIN ? wm * 1024 + lane * 16 : (wrow0 + r16) * 128 + ((q4 ^ ((r16 >> 1) & 7)) << 4), xa1 = XLIN ? xa0 + 2048 : xa0 ^ 64;
  const int wrow = g::XB + (16 * (q4 >> 1) + 4 * (q4 & 1) + (r16 >> 2)) * g::WP + (lane & 3) * 8;
  const int wb0 = wrow + ((odd + jt0) ^ (q4 & 1)) * 32, wb1 = wrow + ((odd + jt0 + 1) ^ (q4 & 1)) * 32;
#pragma unroll
  for (int i = 0; i < g::MT; ++i)
#pragma unroll
    for (int j = 0; j < g::NTW; ++j) acc[i][j] = f32x4{0, 0, 0, 0};
  f32x4 a1[g::MT], a2[g::MT], ac = {0, 0, 0, 0}, ac4 = {0, 0, 0, 0};
#pragma unroll
  for (int i = 0; i < g::MT; ++i) { a1[i] = f32x4{0, 0, 0, 0}; a2[i] = f32x4{0, 0, 0, 0}; }
  const u32x4 onesu = {0x3f803f80u, 0x3f803f80u, 0x3f803f80u, 0x3f803f80u}; const bf16x8 ones = __builtin_bit_cast(bf16x8, onesu);
  auto bar = [&](int s) {
    hook.before(s);
    if (g::LAG == 1) barrier(); else asm volatile("s_barrier" ::: "memory");
    hook.after(s);

  };
  auto rdx = [&](int gk, int i) { return *(const bf16x8*)(smem + g::xs(gk >> 1) + ((gk & 1) ? xa1 : xa0) + i * 2048); };
  auto rdw = [&](int gk, int j) {
    const char* a = smem + g::wsl(gk >> 1) + ((j & 1) ? wb1 + (j - 1) * 32 : wb0 + j * 32) + (32 * (gk & 1)) * g::WP;
    s16x4 lo = __builtin_amdgcn_ds_read_tr16_b64_v4i16((LDS_AS s16x4*)a), hi = __builtin_amdgcn_ds_read_tr16_b64_v4i16((LDS_AS s16x4*)(a + 8 * g::WP));
    return __builtin_bit_cast(bf16x8, __builtin_shufflevector(lo, hi, 0, 1, 2, 3, 4, 5, 6, 7));
  };
  auto mma = [&](bf16x8 xv, bf16x8 wv, f32x4 c) { return TRANS ? __builtin_amdgcn_mfma_f32_16x16x32_bf16(wv, xv, c, 0, 0, 0) : __builtin_amdgcn_mfma_f32_16x16x32_bf16(xv, wv, c, 0, 0, 0); };
  if constexpr (g::MT >= 4 || g::NH == 0) {
    bf16x8 xf[2][g::MT], wf[g::NTW];
    bar(0);
#pragma unroll
    for (int i = 0; i < g::MT; ++i) xf[0][i] = rdx(0, i);
#pragma unroll
    for (int j = 0; j < g::NTW; ++j) wf[j] = rdw(0, j);
#pragma unroll
    for (int gk = 0; gk < 18; ++gk) {
      const int b = gk & 1;
      acc[0][0] = mma(xf[b][0], wf[0], acc[0][0]);
      SBAR();
      if (gk + 1 < 18) {
        if (gk & 1) bar((gk + 1) >> 1);
#pragma unroll
        for (int i = 0; i < g::MT; ++i) xf[b ^ 1][i] = rdx(gk + 1, i);
      }
      SBAR();
#pragma unroll
      for (int j = 0; j < g::NTW; ++j) {
#pragma unroll
        for (int i = 0; i < g::MT; ++i) if (i + j > 0) acc[i][j] = mma(xf[b][i], wf[j], acc[i][j]);
        SBAR();
        if (gk + 1 < 18) wf[j] = rdw(gk + 1, j);
        SBAR();
      }
    }
    return;
  }
  constexpr int NB = g::PD + 1;
  bf16x8 xf[NB][g::MT], wf[NB][g::NTW];
  auto rd = [&](int gk, int b) {
    const char* st = smem + ((gk >> 1) % g::NS) * g::STAGE; const int ks = gk & 1;
#pragma unroll
    for (int i = 0; i < g::MT; ++i) xf[b][i] = *(const bf16x8*)(st + (ks ? xa1 : xa0) + i * 2048);
#pragma unroll
    for (int j = 0; j < g::NTW; ++j) {
      s16x4 lo = __builtin_amdgcn_ds_read_tr16_b64_v4i16((LDS_AS s16x4*)(st + ((j & 1) ? wb1 + (j - 1) * 32 : wb0 + j * 32) + (32 * ks) * g::WP));
      s16x4 hi = __builtin_amdgcn_ds_read_tr16_b64_v4i16((LDS_AS s16x4*)(st + ((j & 1) ? wb1 + (j - 1) * 32 : wb0 + j * 32) + (32 * ks + 8) * g::WP));
      wf[b][j] = __builtin_bit_cast(bf16x8, __builtin_shufflevector(lo, hi, 0, 1, 2, 3, 4, 5, 6, 7));
    }
  };
  auto mm = [&](int b, int i, int j) {
    acc[i][j] = TRANS ? __builtin_amdgcn_mfma_f32_16x16x32_bf16(wf[b][j], xf[b][i], acc[i][j], 0, 0, 0)
                      : __builtin_amdgcn_mfma_f32_16x16x32_bf16(xf[b][i], wf[b][j], acc[i][j], 0, 0, 0);
  };
  bar(0);
#pragma unroll
  for (int gk = 0; gk < g::PD; ++gk) { if (gk > 0 && (gk & 1) == 0) bar(gk >> 1); rd(gk, gk % NB); }
#pragma unroll
  for (int gk = 0; gk < 18; ++gk) {
    const int b = gk % NB;
    mm(b, 0, 0);
    SBAR();
    if (gk + g::PD < 18) {
      if (((gk + g::PD) & 1) == 0) bar((gk + g::PD) >> 1);
      rd(gk + g::PD, (gk + g::PD) % NB);
    }
    SBAR();
#pragma unroll
    for (int j = 0; j < g::NTW; ++j)
#pragma unroll
      for (int i = 0; i < g::MT; ++i) if (i + j > 0) mm(b, i, j);
    if (STATS) {
      if (wn == 0) {
#pragma unroll
        for (int i = 0; i < g::MT; ++i) { a1[i] = __builtin_amdgcn_mfma_f32_16x16x32_bf16(ones, xf[b][i], a1[i], 0, 0, 0); a2[i] = __builtin_amdgcn_mfma_f32_16x16x32_bf16(xf[b][i], xf[b][i], a2[i], 0, 0, 0); }
      }
      if (wm == 0) { ac = __builtin_amdgcn_mfma_f32_16x16x32_bf16(wf[b][0], ones, ac, 0, 0, 0); if (g::NTW == 5) ac4 = __builtin_amdgcn_mfma_f32_16x16x32_bf16(wf[b][g::NTW - 1], ones, ac4, 0, 0, 0); }
      else if (wm == 1) ac = __builtin_amdgcn_mfma_f32_16x16x32_bf16(wf[b][1], ones, ac, 0, 0, 0);
      else if (wm == 2) ac = __builtin_amdgcn_mfma_f32_16x16x32_bf16(wf[b][2], ones, ac, 0, 0, 0);
      else if (g::NTW == 5) ac = __builtin_amdgcn_mfma_f32_16x16x32_bf16(wf[b][g::NTW == 5 ? 3 : 0], ones, ac, 0, 0, 0);
    }
    SBAR();
  }
  if (STATS) {
    if (wn == 0) {
#pragma unroll
      for (int i = 0; i < g::MT; ++i) {
        const int row = wrow0 + 16 * i + r16;
        if (q4 == 0) lst[row * 2] = a1[i][0];
        if (q4 == (r16 >> 2)) { const int e = r16 & 3; lst[row * 2 + 1] = e == 0 ? a2[i][0] : e == 1 ? a2[i][1] : e == 2 ? a2[i][2] : a2[i][3]; }
      }
    }
    if ((wm < 3 || g::NTW == 5) && r16 == 0) *(f32x4*)(lcs + (jt0 + wm) * 16 + 4 * q4) = ac;
    if (g::NTW == 5 && wm == 0 && r16 == 0) *(f32x4*)(lcs + (jt0 + 4) * 16 + 4 * q4) = ac4;
  }
}

#define K1_ROT 0
#define W2_ROT 0
#define K1_NCG 3
#define K1_PD 1
#define K1_AH 2
using G1 = G<128, 2, 2, 4, 4, K1_PD, K1_AH, 4>;
__global__ __launch_bounds__(G1::NW * 64) void k1_qkv(const bf16* __restrict__ x, const bf16* __restrict__ wqkv, const bf16* __restrict__ w1, const bf16* __restrict__ w2,
                                                       const bf16* __restrict__ w3, const bf16* __restrict__ w4, const bf16* __restrict__ wo, char* __restrict__ ws) {
  extern __shared__ __attribute__((aligned(16))) char smem[];
  const int bid = blockIdx.x, xg = bid & 7, idx = bid >> 3;
  char* g_ws_dbg = ws; STAMPK(1, 0);
  if (bid == 0 && threadIdx.x < 32) *(unsigned*)(ws + WS_CNT + threadIdx.x * 128) = 0u;
  const int rt = (xg & 1) * 8 + (idx & 7), ct = (xg >> 1) * 4 + (idx >> 3);
  const int kind = ct >> 2;
  const int w0 = __builtin_amdgcn_readfirstlane(threadIdx.x >> 6), w = w0 < 8 ? w0 + 4 : w0 - 8, wm = w & 1, jt0 = (w >> 1) * G1::JS;
  int lane = threadIdx.x & 63;
  float* lcs = (float*)(smem + G1::STAGE * G1::NS); float* lst = lcs + 288;
  f32x4 acc[G1::MT][G1::NTW]; unsigned sink = 0;
  const int n0 = kind < 3 ? ct * 144 : (ct - 12) * 144;
  const Src sc = {(const char*)x, S_ * D_ * 2, (unsigned)rt * 128 * 1152, (const char*)(kind < 3 ? wqkv : w1), kind < 3 ? 3u * D_ * D_ * 2 : 1u * D_ * D_ * 2,
                  (unsigned)(n0 - (ct & 1) * 16) * 2, kind < 3 ? 3456u : 1152u, K1_ROT};
  if (w >= G1::NC + G1::NH) {
    if (w == G1::NC + G1::NH) {
      const char* cp; int nl; CODE_ADDR(cp, SYM_K1); CODE_LINES(nl, SYM_K1, SYM_K34); int l = idx * 6 + lane; l = lane < 6 ? l : idx * 6;
      const char* a = cp + (l < nl ? l : 0) * 128;
      asm volatile("global_load_dword %0, %1, off" : "+v"(sink) : "v"(a) : "memory");
    }
    gemm_stats<G1>(w - G1::NC - G1::NH, lane, smem, lst, lcs, ct & 1);
  } else if (w >= G1::NC) {
    auto paddr = [&](int i) {
      const int t0 = (w - G1::NC) * 64 + lane;
      if (i == 0) { int l = idx * 234 + t0; l = t0 < 234 ? l : idx * 234;
        return l < 5184 ? (const char*)w2 + (size_t)l * 128 : (const char*)x + (size_t)xg * 256 * 1152 + (size_t)(l - 5184) * 128; }
      if (i == 2) {
        const char* cp; int nl; CODE_ADDR(cp, SYM_K34); CODE_LINES(nl, SYM_K34, "K5_TAIL"); int l = idx * 20 + t0; l = t0 < 20 ? l : idx * 20;
        return cp + (l < nl ? l : 0) * 128; }
      int l = bid * 27 + t0; l = t0 < 27 ? l : bid * 27;
      if (l < 4608) return (const char*)w3 + (size_t)(l >> 3) * 9216 + (l & 7) * 1152;
      l -= 4608; { const int sp = l / 288, r = l - sp * 288; return (const char*)w4 + (size_t)sp * 576 * 1152 + (size_t)r * 128; } };
    gemm_helper<G1, 1, 3, 0>(ws, sc, w - G1::NC, lane, smem, paddr, sink);
  } else {
    if (kind == 2) gemm_consumer<G1, false, false>(acc, w, lane, smem, lst, lcs, ct & 1); else gemm_consumer<G1, true, false>(acc, w, lane, smem, lst, lcs, ct & 1);
  }
  BST(9);
  __syncthreads();
  asm volatile("v_mbcnt_lo_u32_b32 %0, -1, 0\n\tv_mbcnt_hi_u32_b32 %0, -1, %0" : "=v"(lane));
  BST(10);
  const int tid = w0 * 64 + lane, r16 = lane & 15, q4 = lane >> 4;
  if (ct == 0 && tid < 128) { ((float*)(ws + WS_STAT))[rt * 128 + tid] = lst[tid * 4]; ((float*)(ws + WS_STAT))[S_ + rt * 128 + tid] = lst[tid * 4 + 1]; }
  char* img = smem;
  const __amdgpu_buffer_rsrc_t rws = __builtin_amdgcn_make_buffer_rsrc((void*)ws, 0, (unsigned)WS_END, 0x00020000);
#define ST16(p, v) __builtin_amdgcn_raw_buffer_store_b128((v), rws, (int)((const char*)(p) - ws), 0, 16)
  if (w < G1::NC) {
    if (kind != 2) {
      const float sq = kind == 0 ? QSCALE : 1.0f;
      f32x4 c[G1::NTW];
#pragma unroll
      for (int j = 0; j < G1::NTW; ++j) { const int nb = 16 * (jt0 + j) + 4 * q4; c[j] = *(const f32x4*)(lcs + nb) + *(const f32x4*)(lcs + 144 + nb); }
#pragma unroll
      for (int i = 0; i < G1::MT; ++i) {
        const int row = wm * 64 + 16 * i + r16;
        const f32x4 stv = *(const f32x4*)(lst + row * 4); const float rs = kind == 3 ? stv[2] : stv[1] * sq, nb = -stv[0] * rs;
        char* ip = img + row * 304 + (16 * jt0 + 4 * q4) * 2;
#pragma unroll
        for (int j = 0; j < G1::NTW; ++j)
          *(u32x2*)(ip + j * 32) = u32x2{pack2(acc[i][j][0] * rs + nb * c[j][0], acc[i][j][1] * rs + nb * c[j][1]), pack2(acc[i][j][2] * rs + nb * c[j][2], acc[i][j][3] * rs + nb * c[j][3])};
      }
    } else {
      int voff[G1::NTW]; float c[G1::NTW];
#pragma unroll
      for (int j = 0; j < G1::NTW; ++j) { const int n = 16 * (jt0 + j) + r16, hl = n / 18, d = n - hl * 18; c[j] = lcs[n] + lcs[144 + n]; voff[j] = hl * 4864 + d * 16; }
#pragma unroll
      for (int i = 0; i < G1::MT; ++i) {
        const int row0 = wm * 64 + 16 * i + 4 * q4;
        float rs[4], nb[4];
#pragma unroll
        for (int e = 0; e < 4; ++e) { const f32x2 stv = *(const f32x2*)(lst + (row0 + e) * 4); rs[e] = stv[1]; nb[e] = -stv[0] * stv[1]; }
        char* ip = img + (row0 >> 4) * 608 + (q4 & 1) * 304 + (q4 >> 1) * 8;
#pragma unroll
        for (int j = 0; j < G1::NTW; ++j)
          *(u32x2*)(ip + voff[j]) = u32x2{pack2(acc[i][j][0] * rs[0] + nb[0] * c[j], acc[i][j][1] * rs[1] + nb[1] * c[j]), pack2(acc[i][j][2] * rs[2] + nb[2] * c[j], acc[i][j][3] * rs[3] + nb[3] * c[j])};
      }
    }
  } else if (kind == 2 && tid < 128) { const int hl = tid >> 4, sh = tid & 15; *(u32x4*)(img + hl * 4864 + sh * 304 + 18 * 16) = u32x4{0x3f803f80u, 0x3f803f80u, 0x3f803f80u, 0x3f803f80u}; }
  BST(11);
  __syncthreads();
  BST(12);
  if (kind <= 1) {
    const int head0 = (ct & 3) * 8;
#pragma unroll
    for (int i = 0; i < 3; ++i) {
      const int u = tid + 768 * i;
      if (u < 2048) {
        const int hl = u >> 8, kt = (u >> 6) & 3, hh = (u >> 5) & 1, kr = u & 31;
        const char* ip = img + (kt * 32 + kr) * 304 + hl * 36 + hh * 16;
        const u32x4 v = {*(const unsigned*)ip, *(const unsigned*)(ip + 4), *(const unsigned*)(ip + 8), *(const unsigned*)(ip + 12)};
        char* dst = kind == 0 ? ws + WS_Q + ((size_t)(head0 + hl) * 16 + rt) * QRT_B : ws + WS_KV + (size_t)(head0 + hl) * KVHEAD_B + (rt >> 1) * KVBLK_B + (rt & 1) * 4096;
        ST16(dst + (u & 255) * 16, v);
      }
    }
    if (tid < 256) {
      const int hl = tid >> 5, r4 = (tid & 31) * 4;
      const char* ip = img + r4 * 304 + hl * 36 + 32;
      const u32x4 v = {*(const unsigned*)ip, *(const unsigned*)(ip + 304), *(const unsigned*)(ip + 608), *(const unsigned*)(ip + 912)};
      char* dst = kind == 0 ? ws + WS_Q + ((size_t)(head0 + hl) * 16 + rt) * QRT_B + 4096 : ws + WS_KV + (size_t)(head0 + hl) * KVHEAD_B + (rt >> 1) * KVBLK_B + 8192 + (rt & 1) * 512;
      ST16(dst + r4 * 4, v);
    } else if (kind == 1 && tid < 512) {
      const int t2 = tid - 256, hl = t2 >> 5; float mx = 0.f;
#pragma unroll
      for (int i = 0; i < 4; ++i) {
        const char* ip = img + (i * 32 + (t2 & 31)) * 304 + hl * 36; float s = 0.f;
#pragma unroll
        for (int e = 0; e < 9; ++e) { const unsigned u = *(const unsigned*)(ip + 4 * e); s = __builtin_amdgcn_fdot2_f32_bf16(as_bf2(u), as_bf2(u), s, false); }
        mx = fmaxf(mx, s);
      }
#pragma unroll
      for (int o = 1; o < 32; o <<= 1) mx = fmaxf(mx, __shfl_xor(mx, o));
      if ((t2 & 31) == 0) ((float*)(ws + WS_KMAX))[((ct - 4) * 8 + hl) * 16 + rt] = mx;
    }
  } else if (kind == 2) {
    const int head0 = (ct & 3) * 8;
#pragma unroll
    for (int i = 0; i < 4; ++i) {
      const int hl = tid / 96, u = tid - hl * 96 + 96 * i;
      if (u < 304) ST16(ws + WS_KV + (size_t)(head0 + hl) * KVHEAD_B + (rt >> 1) * KVBLK_B + 9216 + (rt & 1) * 4864 + u * 16, *(const u32x4*)(img + hl * 4864 + u * 16));
    }
  } else {
    char* hb = ws + WS_H + (size_t)(2 + rt * 128) * D_ * 2 + (ct - 12) * 288;
#pragma unroll
    for (int i = 0; i < 3; ++i) {
      const int u = tid + 768 * i, row = u / 18, cc = u - row * 18;
      const u32x4 v = *(const u32x4*)(img + row * 304 + cc * 16); u32x4 o;
#pragma unroll
      for (int e = 0; e < 4; ++e) { const unsigned a = v[e]; o[e] = pack2(gelu_tanh(bf_lo(a)), gelu_tanh(bf_hi(a))); }
      ST16(hb + (size_t)row * D_ * 2 + cc * 16, o);
    }
    if ((rt == 0 || rt == 15) && tid < 36) {
      const int row = tid / 18, cc = tid - row * 18;
      *(u32x4*)(ws + WS_H + (size_t)((rt == 0 ? 0 : S_ + 2) + row) * D_ * 2 + (ct - 12) * 288 + cc * 16) = u32x4{0, 0, 0, 0};
    }
  }
  BST(13);
  asm volatile("s_waitcnt vmcnt(0)" : "+v"(sink) :: "memory");
  BST(14);
  STAMPK(1, 4);
}

#define ATT_ASM_TEXT \
  "v_mov_b32 v137, 0x3f80\n\t" \
  "v_mov_b32 v138, 0\n\t" \
  "v_mov_b32 v139, 0\n\t" \
  "v_mov_b32 v141, 0x3f80\n\t" \
  "v_mov_b32 v142, 0\n\t" \
  "v_mov_b32 v143, 0\n\t" \
  "v_mov_b32 v112, 0\n\t" \
  "v_mov_b32 v113, 0\n\t" \
  "v_mov_b32 v114, 0\n\t" \
  "v_mov_b32 v115, 0\n\t" \
  "v_mov_b32 v116, 0\n\t" \
  "v_mov_b32 v117, 0\n\t" \
  "v_mov_b32 v118, 0\n\t" \
  "v_mov_b32 v119, 0\n\t" \
  "v_mov_b32 v152, 0\n\t" \
  "v_mov_b32 v156, 0\n\t" \
  "v_mov_b32 v153, 0\n\t" \
  "v_mov_b32 v157, 0\n\t" \
  "v_mov_b32 v154, 0\n\t" \
  "v_mov_b32 v158, 0\n\t" \
  "v_mov_b32 v155, 0\n\t" \
  "v_mov_b32 v159, 0\n\t" \
  "s_mov_b64 s[42:43], %16\n\t" \
  "s_mov_b32 s44, %17\n\t" \
  "s_waitcnt vmcnt(0)\n\t" \
  "s_barrier\n\t" \
  "global_load_dword v160, %18, off\n\t" \
  "global_load_dword v160, %19, off\n\t" \
  "global_load_dword v160, %20, off\n\t" \
  "ds_read_b128 v[128:131], %4 offset:0\n\t" \
  "ds_read_b32 v136, %5 offset:0\n\t" \
  "ds_read_b128 v[144:147], %6 offset:0\n\t" \
  "ds_read_b128 v[148:151], %6 offset:608\n\t" \
  "s_waitcnt lgkmcnt(2)\n\t" \
  "v_mfma_f32_32x32x16_bf16 v[64:79], v[128:131], %7, 0\n\t" \
  "v_mfma_f32_32x32x16_bf16 v[64:79], v[136:139], %11, v[64:79]\n\t" \
  "s_mov_b32 s40, 0\n\t" \
  "s_nop 7\n\t" \
  "s_nop 7\n\t" \
  "ATT_LOOPA_%=:\n\t" \
  "s_cmp_eq_u32 s40, 1\n\t" \
  "s_cbranch_scc0 ATT_N1_%=\n\t" \
  "s_waitcnt vmcnt(0)\n\t" \
  "s_barrier\n\t" \
  "ATT_N1_%=:\n\t" \
  "v_mfma_f32_32x32x16_bf16 v[80:95], v[128:131], %8, 0\n\t" \
  "ds_read_b128 v[132:135], %4 offset:1024\n\t" \
  "ds_read_b32 v140, %5 offset:128\n\t" \
  "v_exp_f32 v64, v64\n\t" \
  "v_exp_f32 v65, v65\n\t" \
  "v_exp_f32 v66, v66\n\t" \
  "v_exp_f32 v67, v67\n\t" \
  "v_mfma_f32_32x32x16_bf16 v[80:95], v[136:139], %12, v[80:95]\n\t" \
  "v_exp_f32 v68, v68\n\t" \
  "v_exp_f32 v69, v69\n\t" \
  "v_exp_f32 v70, v70\n\t" \
  "v_exp_f32 v71, v71\n\t" \
  "v_cvt_pk_bf16_f32 v64, v64, v65\n\t" \
  "v_cvt_pk_bf16_f32 v65, v66, v67\n\t" \
  "v_mfma_f32_32x32x16_bf16 %3, v[152:155], v[112:115], %3\n\t" \
  "v_exp_f32 v72, v72\n\t" \
  "v_exp_f32 v73, v73\n\t" \
  "v_exp_f32 v74, v74\n\t" \
  "v_exp_f32 v75, v75\n\t" \
  "v_cvt_pk_bf16_f32 v66, v68, v69\n\t" \
  "v_cvt_pk_bf16_f32 v67, v70, v71\n\t" \
  "v_mfma_f32_32x32x16_bf16 %3, v[156:159], v[116:119], %3\n\t" \
  "v_exp_f32 v76, v76\n\t" \
  "v_exp_f32 v77, v77\n\t" \
  "v_exp_f32 v78, v78\n\t" \
  "v_exp_f32 v79, v79\n\t" \
  "v_cvt_pk_bf16_f32 v68, v72, v73\n\t" \
  "v_cvt_pk_bf16_f32 v69, v74, v75\n\t" \
  "v_cvt_pk_bf16_f32 v70, v76, v77\n\t" \
  "v_cvt_pk_bf16_f32 v71, v78, v79\n\t" \
  "v_mfma_f32_32x32x16_bf16 v[96:111], v[128:131], %9, 0\n\t" \
  "s_mov_b32 m0, s44\n\t" \
  "s_add_u32 s44, s44, 4096\n\t" \
  "v_exp_f32 v80, v80\n\t" \
  "v_exp_f32 v81, v81\n\t" \
  "v_exp_f32 v82, v82\n\t" \
  "v_exp_f32 v83, v83\n\t" \
  "v_mfma_f32_32x32x16_bf16 v[96:111], v[136:139], %13, v[96:111]\n\t" \
  "global_load_lds_dwordx4 %15, s[42:43]\n\t" \
  "v_exp_f32 v84, v84\n\t" \
  "v_exp_f32 v85, v85\n\t" \
  "v_exp_f32 v86, v86\n\t" \
  "v_exp_f32 v87, v87\n\t" \
  "v_cvt_pk_bf16_f32 v80, v80, v81\n\t" \
  "v_cvt_pk_bf16_f32 v81, v82, v83\n\t" \
  "s_waitcnt lgkmcnt(2)\n\t" \
  "v_mfma_f32_32x32x16_bf16 %0, v[144:147], v[64:67], %0\n\t" \
  "s_add_u32 s42, s42, 4096\n\t" \
  "s_addc_u32 s43, s43, 0\n\t" \
  "v_exp_f32 v88, v88\n\t" \
  "v_exp_f32 v89, v89\n\t" \
  "v_exp_f32 v90, v90\n\t" \
  "v_exp_f32 v91, v91\n\t" \
  "v_cvt_pk_bf16_f32 v82, v84, v85\n\t" \
  "v_cvt_pk_bf16_f32 v83, v86, v87\n\t" \
  "v_mfma_f32_32x32x16_bf16 %0, v[148:151], v[68:71], %0\n\t" \
  "v_exp_f32 v92, v92\n\t" \
  "v_exp_f32 v93, v93\n\t" \
  "v_exp_f32 v94, v94\n\t" \
  "v_exp_f32 v95, v95\n\t" \
  "v_cvt_pk_bf16_f32 v84, v88, v89\n\t" \
  "v_cvt_pk_bf16_f32 v85, v90, v91\n\t" \
  "v_cvt_pk_bf16_f32 v86, v92, v93\n\t" \
  "v_cvt_pk_bf16_f32 v87, v94, v95\n\t" \
  "v_mfma_f32_32x32x16_bf16 v[112:127], v[128:131], %10, 0\n\t" \
  "v_exp_f32 v96, v96\n\t" \
  "v_exp_f32 v97, v97\n\t" \
  "v_exp_f32 v98, v98\n\t" \
  "v_exp_f32 v99, v99\n\t" \
  "v_mfma_f32_32x32x16_bf16 v[112:127], v[136:139], %14, v[112:127]\n\t" \
  "ds_read_b128 v[152:155], %6 offset:1216\n\t" \
  "ds_read_b128 v[156:159], %6 offset:1824\n\t" \
  "v_exp_f32 v100, v100\n\t" \
  "v_exp_f32 v101, v101\n\t" \
  "v_exp_f32 v102, v102\n\t" \
  "v_exp_f32 v103, v103\n\t" \
  "v_cvt_pk_bf16_f32 v96, v96, v97\n\t" \
  "v_cvt_pk_bf16_f32 v97, v98, v99\n\t" \
  "v_mfma_f32_32x32x16_bf16 %1, v[144:147], v[80:83], %1\n\t" \
  "v_exp_f32 v104, v104\n\t" \
  "v_exp_f32 v105, v105\n\t" \
  "v_exp_f32 v106, v106\n\t" \
  "v_exp_f32 v107, v107\n\t" \
  "v_cvt_pk_bf16_f32 v98, v100, v101\n\t" \
  "v_cvt_pk_bf16_f32 v99, v102, v103\n\t" \
  "v_mfma_f32_32x32x16_bf16 %1, v[148:151], v[84:87], %1\n\t" \
  "v_exp_f32 v108, v108\n\t" \
  "v_exp_f32 v109, v109\n\t" \
  "v_exp_f32 v110, v110\n\t" \
  "v_exp_f32 v111, v111\n\t" \
  "v_cvt_pk_bf16_f32 v100, v104, v105\n\t" \
  "v_cvt_pk_bf16_f32 v101, v106, v107\n\t" \
  "v_cvt_pk_bf16_f32 v102, v108, v109\n\t" \
  "v_cvt_pk_bf16_f32 v103, v110, v111\n\t" \
  "s_waitcnt lgkmcnt(2)\n\t" \
  "v_mfma_f32_32x32x16_bf16 v[64:79], v[132:135], %7, 0\n\t" \
  "v_exp_f32 v112, v112\n\t" \
  "v_exp_f32 v113, v113\n\t" \
  "v_exp_f32 v114, v114\n\t" \
  "v_exp_f32 v115, v115\n\t" \
  "v_mfma_f32_32x32x16_bf16 v[64:79], v[140:143], %11, v[64:79]\n\t" \
  "v_exp_f32 v116, v116\n\t" \
  "v_exp_f32 v117, v117\n\t" \
  "v_exp_f32 v118, v118\n\t" \
  "v_exp_f32 v119, v119\n\t" \
  "v_cvt_pk_bf16_f32 v112, v112, v113\n\t" \
  "v_cvt_pk_bf16_f32 v113, v114, v115\n\t" \
  "v_mfma_f32_32x32x16_bf16 %2, v[144:147], v[96:99], %2\n\t" \
  "v_exp_f32 v120, v120\n\t" \
  "v_exp_f32 v121, v121\n\t" \
  "v_exp_f32 v122, v122\n\t" \
  "v_exp_f32 v123, v123\n\t" \
  "v_cvt_pk_bf16_f32 v114, v116, v117\n\t" \
  "v_cvt_pk_bf16_f32 v115, v118, v119\n\t" \
  "v_mfma_f32_32x32x16_bf16 %2, v[148:151], v[100:103], %2\n\t" \
  "v_exp_f32 v124, v124\n\t" \
  "v_exp_f32 v125, v125\n\t" \
  "v_exp_f32 v126, v126\n\t" \
  "v_exp_f32 v127, v127\n\t" \
  "v_cvt_pk_bf16_f32 v116, v120, v121\n\t" \
  "v_cvt_pk_bf16_f32 v117, v122, v123\n\t" \
  "v_cvt_pk_bf16_f32 v118, v124, v125\n\t" \
  "v_cvt_pk_bf16_f32 v119, v126, v127\n\t" \
  "v_mfma_f32_32x32x16_bf16 v[80:95], v[132:135], %8, 0\n\t" \
  "ds_read_b128 v[128:131], %4 offset:2048\n\t" \
  "ds_read_b32 v136, %5 offset:256\n\t" \
  "s_mov_b32 m0, s44\n\t" \
  "s_add_u32 s44, s44, 4096\n\t" \
  "v_exp_f32 v64, v64\n\t" \
  "v_exp_f32 v65, v65\n\t" \
  "v_exp_f32 v66, v66\n\t" \
  "v_exp_f32 v67, v67\n\t" \
  "v_mfma_f32_32x32x16_bf16 v[80:95], v[140:143], %12, v[80:95]\n\t" \
  "global_load_lds_dwordx4 %15, s[42:43]\n\t" \
  "v_exp_f32 v68, v68\n\t" \
  "v_exp_f32 v69, v69\n\t" \
  "v_exp_f32 v70, v70\n\t" \
  "v_exp_f32 v71, v71\n\t" \
  "v_cvt_pk_bf16_f32 v64, v64, v65\n\t" \
  "v_cvt_pk_bf16_f32 v65, v66, v67\n\t" \
  "v_mfma_f32_32x32x16_bf16 %3, v[144:147], v[112:115], %3\n\t" \
  "s_add_u32 s42, s42, 4096\n\t" \
  "s_addc_u32 s43, s43, 0\n\t" \
  "v_exp_f32 v72, v72\n\t" \
  "v_exp_f32 v73, v73\n\t" \
  "v_exp_f32 v74, v74\n\t" \
  "v_exp_f32 v75, v75\n\t" \
  "v_cvt_pk_bf16_f32 v66, v68, v69\n\t" \
  "v_cvt_pk_bf16_f32 v67, v70, v71\n\t" \
  "v_mfma_f32_32x32x16_bf16 %3, v[148:151], v[116:119], %3\n\t" \
  "v_exp_f32 v76, v76\n\t" \
  "v_exp_f32 v77, v77\n\t" \
  "v_exp_f32 v78, v78\n\t" \
  "v_exp_f32 v79, v79\n\t" \
  "v_cvt_pk_bf16_f32 v68, v72, v73\n\t" \
  "v_cvt_pk_bf16_f32 v69, v74, v75\n\t" \
  "v_cvt_pk_bf16_f32 v70, v76, v77\n\t" \
  "v_cvt_pk_bf16_f32 v71, v78, v79\n\t" \
  "v_mfma_f32_32x32x16_bf16 v[96:111], v[132:135], %9, 0\n\t" \
  "v_exp_f32 v80, v80\n\t" \
  "v_exp_f32 v81, v81\n\t" \
  "v_exp_f32 v82, v82\n\t" \
  "v_exp_f32 v83, v83\n\t" \
  "v_mfma_f32_32x32x16_bf16 v[96:111], v[140:143], %13, v[96:111]\n\t" \
  "v_exp_f32 v84, v84\n\t" \
  "v_exp_f32 v85, v85\n\t" \
  "v_exp_f32 v86, v86\n\t" \
  "v_exp_f32 v87, v87\n\t" \
  "v_cvt_pk_bf16_f32 v80, v80, v81\n\t" \
  "v_cvt_pk_bf16_f32 v81, v82, v83\n\t" \
  "s_waitcnt lgkmcnt(2)\n\t" \
  "v_mfma_f32_32x32x16_bf16 %0, v[152:155], v[64:67], %0\n\t" \
  "v_exp_f32 v88, v88\n\t" \
  "v_exp_f32 v89, v89\n\t" \
  "v_exp_f32 v90, v90\n\t" \
  "v_exp_f32 v91, v91\n\t" \
  "v_cvt_pk_bf16_f32 v82, v84, v85\n\t" \
  "v_cvt_pk_bf16_f32 v83, v86, v87\n\t" \
  "v_mfma_f32_32x32x16_bf16 %0, v[156:159], v[68:71], %0\n\t" \
  "v_exp_f32 v92, v92\n\t" \
  "v_exp_f32 v93, v93\n\t" \
  "v_exp_f32 v94, v94\n\t" \
  "v_exp_f32 v95, v95\n\t" \
  "v_cvt_pk_bf16_f32 v84, v88, v89\n\t" \
  "v_cvt_pk_bf16_f32 v85, v90, v91\n\t" \
  "v_cvt_pk_bf16_f32 v86, v92, v93\n\t" \
  "v_cvt_pk_bf16_f32 v87, v94, v95\n\t" \
  "v_mfma_f32_32x32x16_bf16 v[112:127], v[132:135], %10, 0\n\t" \
  "v_exp_f32 v96, v96\n\t" \
  "v_exp_f32 v97, v97\n\t" \
  "v_exp_f32 v98, v98\n\t" \
  "v_exp_f32 v99, v99\n\t" \
  "v_mfma_f32_32x32x16_bf16 v[112:127], v[140:143], %14, v[112:127]\n\t" \
  "ds_read_b128 v[144:147], %6 offset:2432\n\t" \
  "ds_read_b128 v[148:151], %6 offset:3040\n\t" \
  "v_exp_f32 v100, v100\n\t" \
  "v_exp_f32 v101, v101\n\t" \
  "v_exp_f32 v102, v102\n\t" \
  "v_exp_f32 v103, v103\n\t" \
  "v_cvt_pk_bf16_f32 v96, v96, v97\n\t" \
  "v_cvt_pk_bf16_f32 v97, v98, v99\n\t" \
  "v_mfma_f32_32x32x16_bf16 %1, v[152:155], v[80:83], %1\n\t" \
  "v_exp_f32 v104, v104\n\t" \
  "v_exp_f32 v105, v105\n\t" \
  "v_exp_f32 v106, v106\n\t" \
  "v_exp_f32 v107, v107\n\t" \
  "v_cvt_pk_bf16_f32 v98, v100, v101\n\t" \
  "v_cvt_pk_bf16_f32 v99, v102, v103\n\t" \
  "v_mfma_f32_32x32x16_bf16 %1, v[156:159], v[84:87], %1\n\t" \
  "v_exp_f32 v108, v108\n\t" \
  "v_exp_f32 v109, v109\n\t" \
  "v_exp_f32 v110, v110\n\t" \
  "v_exp_f32 v111, v111\n\t" \
  "v_cvt_pk_bf16_f32 v100, v104, v105\n\t" \
  "v_cvt_pk_bf16_f32 v101, v106, v107\n\t" \
  "v_cvt_pk_bf16_f32 v102, v108, v109\n\t" \
  "v_cvt_pk_bf16_f32 v103, v110, v111\n\t" \
  "s_waitcnt lgkmcnt(2)\n\t" \
  "v_mfma_f32_32x32x16_bf16 v[64:79], v[128:131], %7, 0\n\t" \
  "s_mov_b32 m0, s44\n\t" \
  "s_add_u32 s44, s44, 4096\n\t" \
  "v_exp_f32 v112, v112\n\t" \
  "v_exp_f32 v113, v113\n\t" \
  "v_exp_f32 v114, v114\n\t" \
  "v_exp_f32 v115, v115\n\t" \
  "v_mfma_f32_32x32x16_bf16 v[64:79], v[136:139], %11, v[64:79]\n\t" \
  "global_load_lds_dwordx4 %15, s[42:43]\n\t" \
  "v_exp_f32 v116, v116\n\t" \
  "v_exp_f32 v117, v117\n\t" \
  "v_exp_f32 v118, v118\n\t" \
  "v_exp_f32 v119, v119\n\t" \
  "v_cvt_pk_bf16_f32 v112, v112, v113\n\t" \
  "v_cvt_pk_bf16_f32 v113, v114, v115\n\t" \
  "v_mfma_f32_32x32x16_bf16 %2, v[152:155], v[96:99], %2\n\t" \
  "s_add_u32 s42, s42, 4096\n\t" \
  "s_addc_u32 s43, s43, 0\n\t" \
  "v_exp_f32 v120, v120\n\t" \
  "v_exp_f32 v121, v121\n\t" \
  "v_exp_f32 v122, v122\n\t" \
  "v_exp_f32 v123, v123\n\t" \
  "v_cvt_pk_bf16_f32 v114, v116, v117\n\t" \
  "v_cvt_pk_bf16_f32 v115, v118, v119\n\t" \
  "v_mfma_f32_32x32x16_bf16 %2, v[156:159], v[100:103], %2\n\t" \
  "v_exp_f32 v124, v124\n\t" \
  "v_exp_f32 v125, v125\n\t" \
  "v_exp_f32 v126, v126\n\t" \
  "v_exp_f32 v127, v127\n\t" \
  "v_cvt_pk_bf16_f32 v116, v120, v121\n\t" \
  "v_cvt_pk_bf16_f32 v117, v122, v123\n\t" \
  "v_cvt_pk_bf16_f32 v118, v124, v125\n\t" \
  "v_cvt_pk_bf16_f32 v119, v126, v127\n\t" \
  "v_mfma_f32_32x32x16_bf16 v[80:95], v[128:131], %8, 0\n\t" \
  "ds_read_b128 v[132:135], %4 offset:3072\n\t" \
  "ds_read_b32 v140, %5 offset:384\n\t" \
  "v_exp_f32 v64, v64\n\t" \
  "v_exp_f32 v65, v65\n\t" \
  "v_exp_f32 v66, v66\n\t" \
  "v_exp_f32 v67, v67\n\t" \
  "v_mfma_f32_32x32x16_bf16 v[80:95], v[136:139], %12, v[80:95]\n\t" \
  "v_exp_f32 v68, v68\n\t" \
  "v_exp_f32 v69, v69\n\t" \
  "v_exp_f32 v70, v70\n\t" \
  "v_exp_f32 v71, v71\n\t" \
  "v_cvt_pk_bf16_f32 v64, v64, v65\n\t" \
  "v_cvt_pk_bf16_f32 v65, v66, v67\n\t" \
  "v_mfma_f32_32x32x16_bf16 %3, v[152:155], v[112:115], %3\n\t" \
  "v_exp_f32 v72, v72\n\t" \
  "v_exp_f32 v73, v73\n\t" \
  "v_exp_f32 v74, v74\n\t" \
  "v_exp_f32 v75, v75\n\t" \
  "v_cvt_pk_bf16_f32 v66, v68, v69\n\t" \
  "v_cvt_pk_bf16_f32 v67, v70, v71\n\t" \
  "v_mfma_f32_32x32x16_bf16 %3, v[156:159], v[116:119], %3\n\t" \
  "v_exp_f32 v76, v76\n\t" \
  "v_exp_f32 v77, v77\n\t" \
  "v_exp_f32 v78, v78\n\t" \
  "v_exp_f32 v79, v79\n\t" \
  "v_cvt_pk_bf16_f32 v68, v72, v73\n\t" \
  "v_cvt_pk_bf16_f32 v69, v74, v75\n\t" \
  "v_cvt_pk_bf16_f32 v70, v76, v77\n\t" \
  "v_cvt_pk_bf16_f32 v71, v78, v79\n\t" \
  "v_mfma_f32_32x32x16_bf16 v[96:111], v[128:131], %9, 0\n\t" \
  "v_exp_f32 v80, v80\n\t" \
  "v_exp_f32 v81, v81\n\t" \
  "v_exp_f32 v82, v82\n\t" \
  "v_exp_f32 v83, v83\n\t" \
  "v_mfma_f32_32x32x16_bf16 v[96:111], v[136:139], %13, v[96:111]\n\t" \
  "v_exp_f32 v84, v84\n\t" \
  "v_exp_f32 v85, v85\n\t" \
  "v_exp_f32 v86, v86\n\t" \
  "v_exp_f32 v87, v87\n\t" \
  "v_cvt_pk_bf16_f32 v80, v80, v81\n\t" \
  "v_cvt_pk_bf16_f32 v81, v82, v83\n\t" \
  "s_waitcnt lgkmcnt(2)\n\t" \
  "v_mfma_f32_32x32x16_bf16 %0, v[144:147], v[64:67], %0\n\t" \
  "v_exp_f32 v88, v88\n\t" \
  "v_exp_f32 v89, v89\n\t" \
  "v_exp_f32 v90, v90\n\t" \
  "v_exp_f32 v91, v91\n\t" \
  "v_cvt_pk_bf16_f32 v82, v84, v85\n\t" \
  "v_cvt_pk_bf16_f32 v83, v86, v87\n\t" \
  "v_mfma_f32_32x32x16_bf16 %0, v[148:151], v[68:71], %0\n\t" \
  "v_exp_f32 v92, v92\n\t" \
  "v_exp_f32 v93, v93\n\t" \
  "v_exp_f32 v94, v94\n\t" \
  "v_exp_f32 v95, v95\n\t" \
  "v_cvt_pk_bf16_f32 v84, v88, v89\n\t" \
  "v_cvt_pk_bf16_f32 v85, v90, v91\n\t" \
  "v_cvt_pk_bf16_f32 v86, v92, v93\n\t" \
  "v_cvt_pk_bf16_f32 v87, v94, v95\n\t" \
  "v_mfma_f32_32x32x16_bf16 v[112:127], v[128:131], %10, 0\n\t" \
  "s_mov_b32 m0, s44\n\t" \
  "s_add_u32 s44, s44, 4096\n\t" \
  "v_exp_f32 v96, v96\n\t" \
  "v_exp_f32 v97, v97\n\t" \
  "v_exp_f32 v98, v98\n\t" \
  "v_exp_f32 v99, v99\n\t" \
  "v_mfma_f32_32x32x16_bf16 v[112:127], v[136:139], %14, v[112:127]\n\t" \
  "ds_read_b128 v[152:155], %6 offset:3648\n\t" \
  "ds_read_b128 v[156:159], %6 offset:4256\n\t" \
  "global_load_lds_dwordx4 %15, s[42:43]\n\t" \
  "v_exp_f32 v100, v100\n\t" \
  "v_exp_f32 v101, v101\n\t" \
  "v_exp_f32 v102, v102\n\t" \
  "v_exp_f32 v103, v103\n\t" \
  "v_cvt_pk_bf16_f32 v96, v96, v97\n\t" \
  "v_cvt_pk_bf16_f32 v97, v98, v99\n\t" \
  "v_mfma_f32_32x32x16_bf16 %1, v[144:147], v[80:83], %1\n\t" \
  "s_add_u32 s42, s42, 4096\n\t" \
  "s_addc_u32 s43, s43, 0\n\t" \
  "v_exp_f32 v104, v104\n\t" \
  "v_exp_f32 v105, v105\n\t" \
  "v_exp_f32 v106, v106\n\t" \
  "v_exp_f32 v107, v107\n\t" \
  "v_cvt_pk_bf16_f32 v98, v100, v101\n\t" \
  "v_cvt_pk_bf16_f32 v99, v102, v103\n\t" \
  "v_mfma_f32_32x32x16_bf16 %1, v[148:151], v[84:87], %1\n\t" \
  "v_exp_f32 v108, v108\n\t" \
  "v_exp_f32 v109, v109\n\t" \
  "v_exp_f32 v110, v110\n\t" \
  "v_exp_f32 v111, v111\n\t" \
  "v_cvt_pk_bf16_f32 v100, v104, v105\n\t" \
  "v_cvt_pk_bf16_f32 v101, v106, v107\n\t" \
  "v_cvt_pk_bf16_f32 v102, v108, v109\n\t" \
  "v_cvt_pk_bf16_f32 v103, v110, v111\n\t" \
  "s_waitcnt lgkmcnt(2)\n\t" \
  "v_mfma_f32_32x32x16_bf16 v[64:79], v[132:135], %7, 0\n\t" \
  "v_exp_f32 v112, v112\n\t" \
  "v_exp_f32 v113, v113\n\t" \
  "v_exp_f32 v114, v114\n\t" \
  "v_exp_f32 v115, v115\n\t" \
  "v_mfma_f32_32x32x16_bf16 v[64:79], v[140:143], %11, v[64:79]\n\t" \
  "v_exp_f32 v116, v116\n\t" \
  "v_exp_f32 v117, v117\n\t" \
  "v_exp_f32 v118, v118\n\t" \
  "v_exp_f32 v119, v119\n\t" \
  "v_cvt_pk_bf16_f32 v112, v112, v113\n\t" \
  "v_cvt_pk_bf16_f32 v113, v114, v115\n\t" \
  "v_mfma_f32_32x32x16_bf16 %2, v[144:147], v[96:99], %2\n\t" \
  "v_exp_f32 v120, v120\n\t" \
  "v_exp_f32 v121, v121\n\t" \
  "v_exp_f32 v122, v122\n\t" \
  "v_exp_f32 v123, v123\n\t" \
  "v_cvt_pk_bf16_f32 v114, v116, v117\n\t" \
  "v_cvt_pk_bf16_f32 v115, v118, v119\n\t" \
  "v_mfma_f32_32x32x16_bf16 %2, v[148:151], v[100:103], %2\n\t" \
  "v_exp_f32 v124, v124\n\t" \
  "v_exp_f32 v125, v125\n\t" \
  "v_exp_f32 v126, v126\n\t" \
  "v_exp_f32 v127, v127\n\t" \
  "v_cvt_pk_bf16_f32 v116, v120, v121\n\t" \
  "v_cvt_pk_bf16_f32 v117, v122, v123\n\t" \
  "v_cvt_pk_bf16_f32 v118, v124, v125\n\t" \
  "v_cvt_pk_bf16_f32 v119, v126, v127\n\t" \
  "v_mfma_f32_32x32x16_bf16 v[80:95], v[132:135], %8, 0\n\t" \
  "ds_read_b128 v[128:131], %4 offset:4096\n\t" \
  "ds_read_b32 v136, %5 offset:512\n\t" \
  "v_exp_f32 v64, v64\n\t" \
  "v_exp_f32 v65, v65\n\t" \
  "v_exp_f32 v66, v66\n\t" \
  "v_exp_f32 v67, v67\n\t" \
  "v_mfma_f32_32x32x16_bf16 v[80:95], v[140:143], %12, v[80:95]\n\t" \
  "v_exp_f32 v68, v68\n\t" \
  "v_exp_f32 v69, v69\n\t" \
  "v_exp_f32 v70, v70\n\t" \
  "v_exp_f32 v71, v71\n\t" \
  "v_cvt_pk_bf16_f32 v64, v64, v65\n\t" \
  "v_cvt_pk_bf16_f32 v65, v66, v67\n\t" \
  "v_mfma_f32_32x32x16_bf16 %3, v[144:147], v[112:115], %3\n\t" \
  "v_exp_f32 v72, v72\n\t" \
  "v_exp_f32 v73, v73\n\t" \
  "v_exp_f32 v74, v74\n\t" \
  "v_exp_f32 v75, v75\n\t" \
  "v_cvt_pk_bf16_f32 v66, v68, v69\n\t" \
  "v_cvt_pk_bf16_f32 v67, v70, v71\n\t" \
  "v_mfma_f32_32x32x16_bf16 %3, v[148:151], v[116:119], %3\n\t" \
  "v_exp_f32 v76, v76\n\t" \
  "v_exp_f32 v77, v77\n\t" \
  "v_exp_f32 v78, v78\n\t" \
  "v_exp_f32 v79, v79\n\t" \
  "v_cvt_pk_bf16_f32 v68, v72, v73\n\t" \
  "v_cvt_pk_bf16_f32 v69, v74, v75\n\t" \
  "v_cvt_pk_bf16_f32 v70, v76, v77\n\t" \
  "v_cvt_pk_bf16_f32 v71, v78, v79\n\t" \
  "v_mfma_f32_32x32x16_bf16 v[96:111], v[132:135], %9, 0\n\t" \
  "s_mov_b32 m0, s44\n\t" \
  "s_add_u32 s44, s44, 4096\n\t" \
  "v_exp_f32 v80, v80\n\t" \
  "v_exp_f32 v81, v81\n\t" \
  "v_exp_f32 v82, v82\n\t" \
  "v_exp_f32 v83, v83\n\t" \
  "v_mfma_f32_32x32x16_bf16 v[96:111], v[140:143], %13, v[96:111]\n\t" \
  "global_load_lds_dwordx4 %15, s[42:43]\n\t" \
  "v_exp_f32 v84, v84\n\t" \
  "v_exp_f32 v85, v85\n\t" \
  "v_exp_f32 v86, v86\n\t" \
  "v_exp_f32 v87, v87\n\t" \
  "v_cvt_pk_bf16_f32 v80, v80, v81\n\t" \
  "v_cvt_pk_bf16_f32 v81, v82, v83\n\t" \
  "s_waitcnt lgkmcnt(2)\n\t" \
  "v_mfma_f32_32x32x16_bf16 %0, v[152:155], v[64:67], %0\n\t" \
  "s_add_u32 s42, s42, 4096\n\t" \
  "s_addc_u32 s43, s43, 0\n\t" \
  "v_exp_f32 v88, v88\n\t" \
  "v_exp_f32 v89, v89\n\t" \
  "v_exp_f32 v90, v90\n\t" \
  "v_exp_f32 v91, v91\n\t" \
  "v_cvt_pk_bf16_f32 v82, v84, v85\n\t" \
  "v_cvt_pk_bf16_f32 v83, v86, v87\n\t" \
  "v_mfma_f32_32x32x16_bf16 %0, v[156:159], v[68:71], %0\n\t" \
  "v_exp_f32 v92, v92\n\t" \
  "v_exp_f32 v93, v93\n\t" \
  "v_exp_f32 v94, v94\n\t" \
  "v_exp_f32 v95, v95\n\t" \
  "v_cvt_pk_bf16_f32 v84, v88, v89\n\t" \
  "v_cvt_pk_bf16_f32 v85, v90, v91\n\t" \
  "v_cvt_pk_bf16_f32 v86, v92, v93\n\t" \
  "v_cvt_pk_bf16_f32 v87, v94, v95\n\t" \
  "v_mfma_f32_32x32x16_bf16 v[112:127], v[132:135], %10, 0\n\t" \
  "v_exp_f32 v96, v96\n\t" \
  "v_exp_f32 v97, v97\n\t" \
  "v_exp_f32 v98, v98\n\t" \
  "v_exp_f32 v99, v99\n\t" \
  "v_mfma_f32_32x32x16_bf16 v[112:127], v[140:143], %14, v[112:127]\n\t" \
  "ds_read_b128 v[144:147], %6 offset:4864\n\t" \
  "ds_read_b128 v[148:151], %6 offset:5472\n\t" \
  "v_exp_f32 v100, v100\n\t" \
  "v_exp_f32 v101, v101\n\t" \
  "v_exp_f32 v102, v102\n\t" \
  "v_exp_f32 v103, v103\n\t" \
  "v_cvt_pk_bf16_f32 v96, v96, v97\n\t" \
  "v_cvt_pk_bf16_f32 v97, v98, v99\n\t" \
  "v_mfma_f32_32x32x16_bf16 %1, v[152:155], v[80:83], %1\n\t" \
  "v_exp_f32 v104, v104\n\t" \
  "v_exp_f32 v105, v105\n\t" \
  "v_exp_f32 v106, v106\n\t" \
  "v_exp_f32 v107, v107\n\t" \
  "v_cvt_pk_bf16_f32 v98, v100, v101\n\t" \
  "v_cvt_pk_bf16_f32 v99, v102, v103\n\t" \
  "v_mfma_f32_32x32x16_bf16 %1, v[156:159], v[84:87], %1\n\t" \
  "v_exp_f32 v108, v108\n\t" \
  "v_exp_f32 v109, v109\n\t" \
  "v_exp_f32 v110, v110\n\t" \
  "v_exp_f32 v111, v111\n\t" \
  "v_cvt_pk_bf16_f32 v100, v104, v105\n\t" \
  "v_cvt_pk_bf16_f32 v101, v106, v107\n\t" \
  "v_cvt_pk_bf16_f32 v102, v108, v109\n\t" \
  "v_cvt_pk_bf16_f32 v103, v110, v111\n\t" \
  "s_waitcnt lgkmcnt(2)\n\t" \
  "v_mfma_f32_32x32x16_bf16 v[64:79], v[128:131], %7, 0\n\t" \
  "v_exp_f32 v112, v112\n\t" \
  "v_exp_f32 v113, v113\n\t" \
  "v_exp_f32 v114, v114\n\t" \
  "v_exp_f32 v115, v115\n\t" \
  "v_mfma_f32_32x32x16_bf16 v[64:79], v[136:139], %11, v[64:79]\n\t" \
  "v_exp_f32 v116, v116\n\t" \
  "v_exp_f32 v117, v117\n\t" \
  "v_exp_f32 v118, v118\n\t" \
  "v_exp_f32 v119, v119\n\t" \
  "v_cvt_pk_bf16_f32 v112, v112, v113\n\t" \
  "v_cvt_pk_bf16_f32 v113, v114, v115\n\t" \
  "v_mfma_f32_32x32x16_bf16 %2, v[152:155], v[96:99], %2\n\t" \
  "v_exp_f32 v120, v120\n\t" \
  "v_exp_f32 v121, v121\n\t" \
  "v_exp_f32 v122, v122\n\t" \
  "v_exp_f32 v123, v123\n\t" \
  "v_cvt_pk_bf16_f32 v114, v116, v117\n\t" \
  "v_cvt_pk_bf16_f32 v115, v118, v119\n\t" \
  "v_mfma_f32_32x32x16_bf16 %2, v[156:159], v[100:103], %2\n\t" \
  "v_exp_f32 v124, v124\n\t" \
  "v_exp_f32 v125, v125\n\t" \
  "v_exp_f32 v126, v126\n\t" \
  "v_exp_f32 v127, v127\n\t" \
  "v_cvt_pk_bf16_f32 v116, v120, v121\n\t" \
  "v_cvt_pk_bf16_f32 v117, v122, v123\n\t" \
  "v_cvt_pk_bf16_f32 v118, v124, v125\n\t" \
  "v_cvt_pk_bf16_f32 v119, v126, v127\n\t" \
  "v_mfma_f32_32x32x16_bf16 v[80:95], v[128:131], %8, 0\n\t" \
  "ds_read_b128 v[132:135], %4 offset:5120\n\t" \
  "ds_read_b32 v140, %5 offset:640\n\t" \
  "s_mov_b32 m0, s44\n\t" \
  "s_add_u32 s44, s44, 4096\n\t" \
  "v_exp_f32 v64, v64\n\t" \
  "v_exp_f32 v65, v65\n\t" \
  "v_exp_f32 v66, v66\n\t" \
  "v_exp_f32 v67, v67\n\t" \
  "v_mfma_f32_32x32x16_bf16 v[80:95], v[136:139], %12, v[80:95]\n\t" \
  "global_load_lds_dwordx4 %15, s[42:43]\n\t" \
  "v_exp_f32 v68, v68\n\t" \
  "v_exp_f32 v69, v69\n\t" \
  "v_exp_f32 v70, v70\n\t" \
  "v_exp_f32 v71, v71\n\t" \
  "v_cvt_pk_bf16_f32 v64, v64, v65\n\t" \
  "v_cvt_pk_bf16_f32 v65, v66, v67\n\t" \
  "v_mfma_f32_32x32x16_bf16 %3, v[152:155], v[112:115], %3\n\t" \
  "s_add_u32 s42, s42, 4096\n\t" \
  "s_addc_u32 s43, s43, 0\n\t" \
  "v_exp_f32 v72, v72\n\t" \
  "v_exp_f32 v73, v73\n\t" \
  "v_exp_f32 v74, v74\n\t" \
  "v_exp_f32 v75, v75\n\t" \
  "v_cvt_pk_bf16_f32 v66, v68, v69\n\t" \
  "v_cvt_pk_bf16_f32 v67, v70, v71\n\t" \
  "v_mfma_f32_32x32x16_bf16 %3, v[156:159], v[116:119], %3\n\t" \
  "v_exp_f32 v76, v76\n\t" \
  "v_exp_f32 v77, v77\n\t" \
  "v_exp_f32 v78, v78\n\t" \
  "v_exp_f32 v79, v79\n\t" \
  "v_cvt_pk_bf16_f32 v68, v72, v73\n\t" \
  "v_cvt_pk_bf16_f32 v69, v74, v75\n\t" \
  "v_cvt_pk_bf16_f32 v70, v76, v77\n\t" \
  "v_cvt_pk_bf16_f32 v71, v78, v79\n\t" \
  "v_mfma_f32_32x32x16_bf16 v[96:111], v[128:131], %9, 0\n\t" \
  "v_exp_f32 v80, v80\n\t" \
  "v_exp_f32 v81, v81\n\t" \
  "v_exp_f32 v82, v82\n\t" \
  "v_exp_f32 v83, v83\n\t" \
  "v_mfma_f32_32x32x16_bf16 v[96:111], v[136:139], %13, v[96:111]\n\t" \
  "v_exp_f32 v84, v84\n\t" \
  "v_exp_f32 v85, v85\n\t" \
  "v_exp_f32 v86, v86\n\t" \
  "v_exp_f32 v87, v87\n\t" \
  "v_cvt_pk_bf16_f32 v80, v80, v81\n\t" \
  "v_cvt_pk_bf16_f32 v81, v82, v83\n\t" \
  "s_waitcnt lgkmcnt(2)\n\t" \
  "v_mfma_f32_32x32x16_bf16 %0, v[144:147], v[64:67], %0\n\t" \
  "v_exp_f32 v88, v88\n\t" \
  "v_exp_f32 v89, v89\n\t" \
  "v_exp_f32 v90, v90\n\t" \
  "v_exp_f32 v91, v91\n\t" \
  "v_cvt_pk_bf16_f32 v82, v84, v85\n\t" \
  "v_cvt_pk_bf16_f32 v83, v86, v87\n\t" \
  "v_mfma_f32_32x32x16_bf16 %0, v[148:151], v[68:71], %0\n\t" \
  "v_exp_f32 v92, v92\n\t" \
  "v_exp_f32 v93, v93\n\t" \
  "v_exp_f32 v94, v94\n\t" \
  "v_exp_f32 v95, v95\n\t" \
  "v_cvt_pk_bf16_f32 v84, v88, v89\n\t" \
  "v_cvt_pk_bf16_f32 v85, v90, v91\n\t" \
  "v_cvt_pk_bf16_f32 v86, v92, v93\n\t" \
  "v_cvt_pk_bf16_f32 v87, v94, v95\n\t" \
  "v_mfma_f32_32x32x16_bf16 v[112:127], v[128:131], %10, 0\n\t" \
  "v_exp_f32 v96, v96\n\t" \
  "v_exp_f32 v97, v97\n\t" \
  "v_exp_f32 v98, v98\n\t" \
  "v_exp_f32 v99, v99\n\t" \
  "v_mfma_f32_32x32x16_bf16 v[112:127], v[136:139], %14, v[112:127]\n\t" \
  "ds_read_b128 v[152:155], %6 offset:6080\n\t" \
  "ds_read_b128 v[156:159], %6 offset:6688\n\t" \
  "v_exp_f32 v100, v100\n\t" \
  "v_exp_f32 v101, v101\n\t" \
  "v_exp_f32 v102, v102\n\t" \
  "v_exp_f32 v103, v103\n\t" \
  "v_cvt_pk_bf16_f32 v96, v96, v97\n\t" \
  "v_cvt_pk_bf16_f32 v97, v98, v99\n\t" \
  "v_mfma_f32_32x32x16_bf16 %1, v[144:147], v[80:83], %1\n\t" \
  "v_exp_f32 v104, v104\n\t" \
  "v_exp_f32 v105, v105\n\t" \
  "v_exp_f32 v106, v106\n\t" \
  "v_exp_f32 v107, v107\n\t" \
  "v_cvt_pk_bf16_f32 v98, v100, v101\n\t" \
  "v_cvt_pk_bf16_f32 v99, v102, v103\n\t" \
  "v_mfma_f32_32x32x16_bf16 %1, v[148:151], v[84:87], %1\n\t" \
  "v_exp_f32 v108, v108\n\t" \
  "v_exp_f32 v109, v109\n\t" \
  "v_exp_f32 v110, v110\n\t" \
  "v_exp_f32 v111, v111\n\t" \
  "v_cvt_pk_bf16_f32 v100, v104, v105\n\t" \
  "v_cvt_pk_bf16_f32 v101, v106, v107\n\t" \
  "v_cvt_pk_bf16_f32 v102, v108, v109\n\t" \
  "v_cvt_pk_bf16_f32 v103, v110, v111\n\t" \
  "s_waitcnt lgkmcnt(2)\n\t" \
  "v_mfma_f32_32x32x16_bf16 v[64:79], v[132:135], %7, 0\n\t" \
  "s_mov_b32 m0, s44\n\t" \
  "s_add_u32 s44, s44, 4096\n\t" \
  "v_exp_f32 v112, v112\n\t" \
  "v_exp_f32 v113, v113\n\t" \
  "v_exp_f32 v114, v114\n\t" \
  "v_exp_f32 v115, v115\n\t" \
  "v_mfma_f32_32x32x16_bf16 v[64:79], v[140:143], %11, v[64:79]\n\t" \
  "global_load_lds_dwordx4 %15, s[42:43]\n\t" \
  "v_exp_f32 v116, v116\n\t" \
  "v_exp_f32 v117, v117\n\t" \
  "v_exp_f32 v118, v118\n\t" \
  "v_exp_f32 v119, v119\n\t" \
  "v_cvt_pk_bf16_f32 v112, v112, v113\n\t" \
  "v_cvt_pk_bf16_f32 v113, v114, v115\n\t" \
  "v_mfma_f32_32x32x16_bf16 %2, v[144:147], v[96:99], %2\n\t" \
  "s_add_u32 s42, s42, 4096\n\t" \
  "s_addc_u32 s43, s43, 0\n\t" \
  "v_exp_f32 v120, v120\n\t" \
  "v_exp_f32 v121, v121\n\t" \
  "v_exp_f32 v122, v122\n\t" \
  "v_exp_f32 v123, v123\n\t" \
  "v_cvt_pk_bf16_f32 v114, v116, v117\n\t" \
  "v_cvt_pk_bf16_f32 v115, v118, v119\n\t" \
  "v_mfma_f32_32x32x16_bf16 %2, v[148:151], v[100:103], %2\n\t" \
  "v_exp_f32 v124, v124\n\t" \
  "v_exp_f32 v125, v125\n\t" \
  "v_exp_f32 v126, v126\n\t" \
  "v_exp_f32 v127, v127\n\t" \
  "v_cvt_pk_bf16_f32 v116, v120, v121\n\t" \
  "v_cvt_pk_bf16_f32 v117, v122, v123\n\t" \
  "v_cvt_pk_bf16_f32 v118, v124, v125\n\t" \
  "v_cvt_pk_bf16_f32 v119, v126, v127\n\t" \
  "v_mfma_f32_32x32x16_bf16 v[80:95], v[132:135], %8, 0\n\t" \
  "ds_read_b128 v[128:131], %4 offset:6144\n\t" \
  "ds_read_b32 v136, %5 offset:768\n\t" \
  "v_exp_f32 v64, v64\n\t" \
  "v_exp_f32 v65, v65\n\t" \
  "v_exp_f32 v66, v66\n\t" \
  "v_exp_f32 v67, v67\n\t" \
  "v_mfma_f32_32x32x16_bf16 v[80:95], v[140:143], %12, v[80:95]\n\t" \
  "v_exp_f32 v68, v68\n\t" \
  "v_exp_f32 v69, v69\n\t" \
  "v_exp_f32 v70, v70\n\t" \
  "v_exp_f32 v71, v71\n\t" \
  "v_cvt_pk_bf16_f32 v64, v64, v65\n\t" \
  "v_cvt_pk_bf16_f32 v65, v66, v67\n\t" \
  "v_mfma_f32_32x32x16_bf16 %3, v[144:147], v[112:115], %3\n\t" \
  "v_exp_f32 v72, v72\n\t" \
  "v_exp_f32 v73, v73\n\t" \
  "v_exp_f32 v74, v74\n\t" \
  "v_exp_f32 v75, v75\n\t" \
  "v_cvt_pk_bf16_f32 v66, v68, v69\n\t" \
  "v_cvt_pk_bf16_f32 v67, v70, v71\n\t" \
  "v_mfma_f32_32x32x16_bf16 %3, v[148:151], v[116:119], %3\n\t" \
  "v_exp_f32 v76, v76\n\t" \
  "v_exp_f32 v77, v77\n\t" \
  "v_exp_f32 v78, v78\n\t" \
  "v_exp_f32 v79, v79\n\t" \
  "v_cvt_pk_bf16_f32 v68, v72, v73\n\t" \
  "v_cvt_pk_bf16_f32 v69, v74, v75\n\t" \
  "v_cvt_pk_bf16_f32 v70, v76, v77\n\t" \
  "v_cvt_pk_bf16_f32 v71, v78, v79\n\t" \
  "v_mfma_f32_32x32x16_bf16 v[96:111], v[132:135], %9, 0\n\t" \
  "v_exp_f32 v80, v80\n\t" \
  "v_exp_f32 v81, v81\n\t" \
  "v_exp_f32 v82, v82\n\t" \
  "v_exp_f32 v83, v83\n\t" \
  "v_mfma_f32_32x32x16_bf16 v[96:111], v[140:143], %13, v[96:111]\n\t" \
  "v_exp_f32 v84, v84\n\t" \
  "v_exp_f32 v85, v85\n\t" \
  "v_exp_f32 v86, v86\n\t" \
  "v_exp_f32 v87, v87\n\t" \
  "v_cvt_pk_bf16_f32 v80, v80, v81\n\t" \
  "v_cvt_pk_bf16_f32 v81, v82, v83\n\t" \
  "s_waitcnt lgkmcnt(2)\n\t" \
  "v_mfma_f32_32x32x16_bf16 %0, v[152:155], v[64:67], %0\n\t" \
  "v_exp_f32 v88, v88\n\t" \
  "v_exp_f32 v89, v89\n\t" \
  "v_exp_f32 v90, v90\n\t" \
  "v_exp_f32 v91, v91\n\t" \
  "v_cvt_pk_bf16_f32 v82, v84, v85\n\t" \
  "v_cvt_pk_bf16_f32 v83, v86, v87\n\t" \
  "v_mfma_f32_32x32x16_bf16 %0, v[156:159], v[68:71], %0\n\t" \
  "v_exp_f32 v92, v92\n\t" \
  "v_exp_f32 v93, v93\n\t" \
  "v_exp_f32 v94, v94\n\t" \
  "v_exp_f32 v95, v95\n\t" \
  "v_cvt_pk_bf16_f32 v84, v88, v89\n\t" \
  "v_cvt_pk_bf16_f32 v85, v90, v91\n\t" \
  "v_cvt_pk_bf16_f32 v86, v92, v93\n\t" \
  "v_cvt_pk_bf16_f32 v87, v94, v95\n\t" \
  "v_mfma_f32_32x32x16_bf16 v[112:127], v[132:135], %10, 0\n\t" \
  "s_mov_b32 m0, s44\n\t" \
  "s_add_u32 s44, s44, 4096\n\t" \
  "v_exp_f32 v96, v96\n\t" \
  "v_exp_f32 v97, v97\n\t" \
  "v_exp_f32 v98, v98\n\t" \
  "v_exp_f32 v99, v99\n\t" \
  "v_mfma_f32_32x32x16_bf16 v[112:127], v[140:143], %14, v[112:127]\n\t" \
  "ds_read_b128 v[144:147], %6 offset:7296\n\t" \
  "ds_read_b128 v[148:151], %6 offset:7904\n\t" \
  "global_load_lds_dwordx4 %15, s[42:43]\n\t" \
  "v_exp_f32 v100, v100\n\t" \
  "v_exp_f32 v101, v101\n\t" \
  "v_exp_f32 v102, v102\n\t" \
  "v_exp_f32 v103, v103\n\t" \
  "v_cvt_pk_bf16_f32 v96, v96, v97\n\t" \
  "v_cvt_pk_bf16_f32 v97, v98, v99\n\t" \
  "v_mfma_f32_32x32x16_bf16 %1, v[152:155], v[80:83], %1\n\t" \
  "s_add_u32 s42, s42, 4096\n\t" \
  "s_addc_u32 s43, s43, 0\n\t" \
  "v_exp_f32 v104, v104\n\t" \
  "v_exp_f32 v105, v105\n\t" \
  "v_exp_f32 v106, v106\n\t" \
  "v_exp_f32 v107, v107\n\t" \
  "v_cvt_pk_bf16_f32 v98, v100, v101\n\t" \
  "v_cvt_pk_bf16_f32 v99, v102, v103\n\t" \
  "v_mfma_f32_32x32x16_bf16 %1, v[156:159], v[84:87], %1\n\t" \
  "v_exp_f32 v108, v108\n\t" \
  "v_exp_f32 v109, v109\n\t" \
  "v_exp_f32 v110, v110\n\t" \
  "v_exp_f32 v111, v111\n\t" \
  "v_cvt_pk_bf16_f32 v100, v104, v105\n\t" \
  "v_cvt_pk_bf16_f32 v101, v106, v107\n\t" \
  "v_cvt_pk_bf16_f32 v102, v108, v109\n\t" \
  "v_cvt_pk_bf16_f32 v103, v110, v111\n\t" \
  "s_waitcnt lgkmcnt(2)\n\t" \
  "v_mfma_f32_32x32x16_bf16 v[64:79], v[128:131], %7, 0\n\t" \
  "v_exp_f32 v112, v112\n\t" \
  "v_exp_f32 v113, v113\n\t" \
  "v_exp_f32 v114, v114\n\t" \
  "v_exp_f32 v115, v115\n\t" \
  "v_mfma_f32_32x32x16_bf16 v[64:79], v[136:139], %11, v[64:79]\n\t" \
  "v_exp_f32 v116, v116\n\t" \
  "v_exp_f32 v117, v117\n\t" \
  "v_exp_f32 v118, v118\n\t" \
  "v_exp_f32 v119, v119\n\t" \
  "v_cvt_pk_bf16_f32 v112, v112, v113\n\t" \
  "v_cvt_pk_bf16_f32 v113, v114, v115\n\t" \
  "v_mfma_f32_32x32x16_bf16 %2, v[152:155], v[96:99], %2\n\t" \
  "v_exp_f32 v120, v120\n\t" \
  "v_exp_f32 v121, v121\n\t" \
  "v_exp_f32 v122, v122\n\t" \
  "v_exp_f32 v123, v123\n\t" \
  "v_cvt_pk_bf16_f32 v114, v116, v117\n\t" \
  "v_cvt_pk_bf16_f32 v115, v118, v119\n\t" \
  "v_mfma_f32_32x32x16_bf16 %2, v[156:159], v[100:103], %2\n\t" \
  "v_exp_f32 v124, v124\n\t" \
  "v_exp_f32 v125, v125\n\t" \
  "v_exp_f32 v126, v126\n\t" \
  "v_exp_f32 v127, v127\n\t" \
  "v_cvt_pk_bf16_f32 v116, v120, v121\n\t" \
  "v_cvt_pk_bf16_f32 v117, v122, v123\n\t" \
  "v_cvt_pk_bf16_f32 v118, v124, v125\n\t" \
  "v_cvt_pk_bf16_f32 v119, v126, v127\n\t" \
  "v_mfma_f32_32x32x16_bf16 v[80:95], v[128:131], %8, 0\n\t" \
  "ds_read_b128 v[132:135], %4 offset:7168\n\t" \
  "ds_read_b32 v140, %5 offset:896\n\t" \
  "v_exp_f32 v64, v64\n\t" \
  "v_exp_f32 v65, v65\n\t" \
  "v_exp_f32 v66, v66\n\t" \
  "v_exp_f32 v67, v67\n\t" \
  "v_mfma_f32_32x32x16_bf16 v[80:95], v[136:139], %12, v[80:95]\n\t" \
  "v_exp_f32 v68, v68\n\t" \
  "v_exp_f32 v69, v69\n\t" \
  "v_exp_f32 v70, v70\n\t" \
  "v_exp_f32 v71, v71\n\t" \
  "v_cvt_pk_bf16_f32 v64, v64, v65\n\t" \
  "v_cvt_pk_bf16_f32 v65, v66, v67\n\t" \
  "v_mfma_f32_32x32x16_bf16 %3, v[152:155], v[112:115], %3\n\t" \
  "v_exp_f32 v72, v72\n\t" \
  "v_exp_f32 v73, v73\n\t" \
  "v_exp_f32 v74, v74\n\t" \
  "v_exp_f32 v75, v75\n\t" \
  "v_cvt_pk_bf16_f32 v66, v68, v69\n\t" \
  "v_cvt_pk_bf16_f32 v67, v70, v71\n\t" \
  "v_mfma_f32_32x32x16_bf16 %3, v[156:159], v[116:119], %3\n\t" \
  "v_exp_f32 v76, v76\n\t" \
  "v_exp_f32 v77, v77\n\t" \
  "v_exp_f32 v78, v78\n\t" \
  "v_exp_f32 v79, v79\n\t" \
  "v_cvt_pk_bf16_f32 v68, v72, v73\n\t" \
  "v_cvt_pk_bf16_f32 v69, v74, v75\n\t" \
  "v_cvt_pk_bf16_f32 v70, v76, v77\n\t" \
  "v_cvt_pk_bf16_f32 v71, v78, v79\n\t" \
  "v_mfma_f32_32x32x16_bf16 v[96:111], v[128:131], %9, 0\n\t" \
  "s_mov_b32 m0, s44\n\t" \
  "s_add_u32 s44, s44, 4096\n\t" \
  "v_exp_f32 v80, v80\n\t" \
  "v_exp_f32 v81, v81\n\t" \
  "v_exp_f32 v82, v82\n\t" \
  "v_exp_f32 v83, v83\n\t" \
  "v_mfma_f32_32x32x16_bf16 v[96:111], v[136:139], %13, v[96:111]\n\t" \
  "global_load_lds_dwordx4 %15, s[42:43]\n\t" \
  "v_exp_f32 v84, v84\n\t" \
  "v_exp_f32 v85, v85\n\t" \
  "v_exp_f32 v86, v86\n\t" \
  "v_exp_f32 v87, v87\n\t" \
  "v_cvt_pk_bf16_f32 v80, v80, v81\n\t" \
  "v_cvt_pk_bf16_f32 v81, v82, v83\n\t" \
  "s_waitcnt lgkmcnt(2)\n\t" \
  "v_mfma_f32_32x32x16_bf16 %0, v[144:147], v[64:67], %0\n\t" \
  "s_add_u32 s42, s42, 4096\n\t" \
  "s_addc_u32 s43, s43, 0\n\t" \
  "v_exp_f32 v88, v88\n\t" \
  "v_exp_f32 v89, v89\n\t" \
  "v_exp_f32 v90, v90\n\t" \
  "v_exp_f32 v91, v91\n\t" \
  "v_cvt_pk_bf16_f32 v82, v84, v85\n\t" \
  "v_cvt_pk_bf16_f32 v83, v86, v87\n\t" \
  "v_mfma_f32_32x32x16_bf16 %0, v[148:151], v[68:71], %0\n\t" \
  "v_exp_f32 v92, v92\n\t" \
  "v_exp_f32 v93, v93\n\t" \
  "v_exp_f32 v94, v94\n\t" \
  "v_exp_f32 v95, v95\n\t" \
  "v_cvt_pk_bf16_f32 v84, v88, v89\n\t" \
  "v_cvt_pk_bf16_f32 v85, v90, v91\n\t" \
  "v_cvt_pk_bf16_f32 v86, v92, v93\n\t" \
  "v_cvt_pk_bf16_f32 v87, v94, v95\n\t" \
  "v_mfma_f32_32x32x16_bf16 v[112:127], v[128:131], %10, 0\n\t" \
  "v_exp_f32 v96, v96\n\t" \
  "v_exp_f32 v97, v97\n\t" \
  "v_exp_f32 v98, v98\n\t" \
  "v_exp_f32 v99, v99\n\t" \
  "v_mfma_f32_32x32x16_bf16 v[112:127], v[136:139], %14, v[112:127]\n\t" \
  "ds_read_b128 v[152:155], %6 offset:8512\n\t" \
  "ds_read_b128 v[156:159], %6 offset:9120\n\t" \
  "v_exp_f32 v100, v100\n\t" \
  "v_exp_f32 v101, v101\n\t" \
  "v_exp_f32 v102, v102\n\t" \
  "v_exp_f32 v103, v103\n\t" \
  "v_cvt_pk_bf16_f32 v96, v96, v97\n\t" \
  "v_cvt_pk_bf16_f32 v97, v98, v99\n\t" \
  "v_mfma_f32_32x32x16_bf16 %1, v[144:147], v[80:83], %1\n\t" \
  "v_exp_f32 v104, v104\n\t" \
  "v_exp_f32 v105, v105\n\t" \
  "v_exp_f32 v106, v106\n\t" \
  "v_exp_f32 v107, v107\n\t" \
  "v_cvt_pk_bf16_f32 v98, v100, v101\n\t" \
  "v_cvt_pk_bf16_f32 v99, v102, v103\n\t" \
  "v_mfma_f32_32x32x16_bf16 %1, v[148:151], v[84:87], %1\n\t" \
  "v_exp_f32 v108, v108\n\t" \
  "v_exp_f32 v109, v109\n\t" \
  "v_exp_f32 v110, v110\n\t" \
  "v_exp_f32 v111, v111\n\t" \
  "v_cvt_pk_bf16_f32 v100, v104, v105\n\t" \
  "v_cvt_pk_bf16_f32 v101, v106, v107\n\t" \
  "v_cvt_pk_bf16_f32 v102, v108, v109\n\t" \
  "v_cvt_pk_bf16_f32 v103, v110, v111\n\t" \
  "s_waitcnt lgkmcnt(2)\n\t" \
  "v_mfma_f32_32x32x16_bf16 v[64:79], v[132:135], %7, 0\n\t" \
  "v_exp_f32 v112, v112\n\t" \
  "v_exp_f32 v113, v113\n\t" \
  "v_exp_f32 v114, v114\n\t" \
  "v_exp_f32 v115, v115\n\t" \
  "v_mfma_f32_32x32x16_bf16 v[64:79], v[140:143], %11, v[64:79]\n\t" \
  "v_exp_f32 v116, v116\n\t" \
  "v_exp_f32 v117, v117\n\t" \
  "v_exp_f32 v118, v118\n\t" \
  "v_exp_f32 v119, v119\n\t" \
  "v_cvt_pk_bf16_f32 v112, v112, v113\n\t" \
  "v_cvt_pk_bf16_f32 v113, v114, v115\n\t" \
  "v_mfma_f32_32x32x16_bf16 %2, v[144:147], v[96:99], %2\n\t" \
  "v_exp_f32 v120, v120\n\t" \
  "v_exp_f32 v121, v121\n\t" \
  "v_exp_f32 v122, v122\n\t" \
  "v_exp_f32 v123, v123\n\t" \
  "v_cvt_pk_bf16_f32 v114, v116, v117\n\t" \
  "v_cvt_pk_bf16_f32 v115, v118, v119\n\t" \
  "v_mfma_f32_32x32x16_bf16 %2, v[148:151], v[100:103], %2\n\t" \
  "v_exp_f32 v124, v124\n\t" \
  "v_exp_f32 v125, v125\n\t" \
  "v_exp_f32 v126, v126\n\t" \
  "v_exp_f32 v127, v127\n\t" \
  "v_cvt_pk_bf16_f32 v116, v120, v121\n\t" \
  "v_cvt_pk_bf16_f32 v117, v122, v123\n\t" \
  "v_cvt_pk_bf16_f32 v118, v124, v125\n\t" \
  "v_cvt_pk_bf16_f32 v119, v126, v127\n\t" \
  "v_mfma_f32_32x32x16_bf16 v[80:95], v[132:135], %8, 0\n\t" \
  "ds_read_b128 v[128:131], %4 offset:18944\n\t" \
  "ds_read_b32 v136, %5 offset:18944\n\t" \
  "v_exp_f32 v64, v64\n\t" \
  "v_exp_f32 v65, v65\n\t" \
  "v_exp_f32 v66, v66\n\t" \
  "v_exp_f32 v67, v67\n\t" \
  "v_mfma_f32_32x32x16_bf16 v[80:95], v[140:143], %12, v[80:95]\n\t" \
  "v_exp_f32 v68, v68\n\t" \
  "v_exp_f32 v69, v69\n\t" \
  "v_exp_f32 v70, v70\n\t" \
  "v_exp_f32 v71, v71\n\t" \
  "v_cvt_pk_bf16_f32 v64, v64, v65\n\t" \
  "v_cvt_pk_bf16_f32 v65, v66, v67\n\t" \
  "v_mfma_f32_32x32x16_bf16 %3, v[144:147], v[112:115], %3\n\t" \
  "v_exp_f32 v72, v72\n\t" \
  "v_exp_f32 v73, v73\n\t" \
  "v_exp_f32 v74, v74\n\t" \
  "v_exp_f32 v75, v75\n\t" \
  "v_cvt_pk_bf16_f32 v66, v68, v69\n\t" \
  "v_cvt_pk_bf16_f32 v67, v70, v71\n\t" \
  "v_mfma_f32_32x32x16_bf16 %3, v[148:151], v[116:119], %3\n\t" \
  "v_exp_f32 v76, v76\n\t" \
  "v_exp_f32 v77, v77\n\t" \
  "v_exp_f32 v78, v78\n\t" \
  "v_exp_f32 v79, v79\n\t" \
  "v_cvt_pk_bf16_f32 v68, v72, v73\n\t" \
  "v_cvt_pk_bf16_f32 v69, v74, v75\n\t" \
  "v_cvt_pk_bf16_f32 v70, v76, v77\n\t" \
  "v_cvt_pk_bf16_f32 v71, v78, v79\n\t" \
  "v_mfma_f32_32x32x16_bf16 v[96:111], v[132:135], %9, 0\n\t" \
  "v_exp_f32 v80, v80\n\t" \
  "v_exp_f32 v81, v81\n\t" \
  "v_exp_f32 v82, v82\n\t" \
  "v_exp_f32 v83, v83\n\t" \
  "v_mfma_f32_32x32x16_bf16 v[96:111], v[140:143], %13, v[96:111]\n\t" \
  "v_exp_f32 v84, v84\n\t" \
  "v_exp_f32 v85, v85\n\t" \
  "v_exp_f32 v86, v86\n\t" \
  "v_exp_f32 v87, v87\n\t" \
  "v_cvt_pk_bf16_f32 v80, v80, v81\n\t" \
  "v_cvt_pk_bf16_f32 v81, v82, v83\n\t" \
  "s_waitcnt lgkmcnt(2)\n\t" \
  "v_mfma_f32_32x32x16_bf16 %0, v[152:155], v[64:67], %0\n\t" \
  "v_exp_f32 v88, v88\n\t" \
  "v_exp_f32 v89, v89\n\t" \
  "v_exp_f32 v90, v90\n\t" \
  "v_exp_f32 v91, v91\n\t" \
  "v_cvt_pk_bf16_f32 v82, v84, v85\n\t" \
  "v_cvt_pk_bf16_f32 v83, v86, v87\n\t" \
  "v_mfma_f32_32x32x16_bf16 %0, v[156:159], v[68:71], %0\n\t" \
  "v_exp_f32 v92, v92\n\t" \
  "v_exp_f32 v93, v93\n\t" \
  "v_exp_f32 v94, v94\n\t" \
  "v_exp_f32 v95, v95\n\t" \
  "v_cvt_pk_bf16_f32 v84, v88, v89\n\t" \
  "v_cvt_pk_bf16_f32 v85, v90, v91\n\t" \
  "v_cvt_pk_bf16_f32 v86, v92, v93\n\t" \
  "v_cvt_pk_bf16_f32 v87, v94, v95\n\t" \
  "v_mfma_f32_32x32x16_bf16 v[112:127], v[132:135], %10, 0\n\t" \
  "v_exp_f32 v96, v96\n\t" \
  "v_exp_f32 v97, v97\n\t" \
  "v_exp_f32 v98, v98\n\t" \
  "v_exp_f32 v99, v99\n\t" \
  "v_mfma_f32_32x32x16_bf16 v[112:127], v[140:143], %14, v[112:127]\n\t" \
  "ds_read_b128 v[144:147], %6 offset:18944\n\t" \
  "ds_read_b128 v[148:151], %6 offset:19552\n\t" \
  "v_exp_f32 v100, v100\n\t" \
  "v_exp_f32 v101, v101\n\t" \
  "v_exp_f32 v102, v102\n\t" \
  "v_exp_f32 v103, v103\n\t" \
  "v_cvt_pk_bf16_f32 v96, v96, v97\n\t" \
  "v_cvt_pk_bf16_f32 v97, v98, v99\n\t" \
  "v_mfma_f32_32x32x16_bf16 %1, v[152:155], v[80:83], %1\n\t" \
  "v_exp_f32 v104, v104\n\t" \
  "v_exp_f32 v105, v105\n\t" \
  "v_exp_f32 v106, v106\n\t" \
  "v_exp_f32 v107, v107\n\t" \
  "v_cvt_pk_bf16_f32 v98, v100, v101\n\t" \
  "v_cvt_pk_bf16_f32 v99, v102, v103\n\t" \
  "v_mfma_f32_32x32x16_bf16 %1, v[156:159], v[84:87], %1\n\t" \
  "v_exp_f32 v108, v108\n\t" \
  "v_exp_f32 v109, v109\n\t" \
  "v_exp_f32 v110, v110\n\t" \
  "v_exp_f32 v111, v111\n\t" \
  "v_cvt_pk_bf16_f32 v100, v104, v105\n\t" \
  "v_cvt_pk_bf16_f32 v101, v106, v107\n\t" \
  "v_cvt_pk_bf16_f32 v102, v108, v109\n\t" \
  "v_cvt_pk_bf16_f32 v103, v110, v111\n\t" \
  "s_waitcnt lgkmcnt(2)\n\t" \
  "v_mfma_f32_32x32x16_bf16 v[64:79], v[128:131], %7, 0\n\t" \
  "v_exp_f32 v112, v112\n\t" \
  "v_exp_f32 v113, v113\n\t" \
  "v_exp_f32 v114, v114\n\t" \
  "v_exp_f32 v115, v115\n\t" \
  "v_mfma_f32_32x32x16_bf16 v[64:79], v[136:139], %11, v[64:79]\n\t" \
  "v_exp_f32 v116, v116\n\t" \
  "v_exp_f32 v117, v117\n\t" \
  "v_exp_f32 v118, v118\n\t" \
  "v_exp_f32 v119, v119\n\t" \
  "v_cvt_pk_bf16_f32 v112, v112, v113\n\t" \
  "v_cvt_pk_bf16_f32 v113, v114, v115\n\t" \
  "v_mfma_f32_32x32x16_bf16 %2, v[152:155], v[96:99], %2\n\t" \
  "v_exp_f32 v120, v120\n\t" \
  "v_exp_f32 v121, v121\n\t" \
  "v_exp_f32 v122, v122\n\t" \
  "v_exp_f32 v123, v123\n\t" \
  "v_cvt_pk_bf16_f32 v114, v116, v117\n\t" \
  "v_cvt_pk_bf16_f32 v115, v118, v119\n\t" \
  "v_mfma_f32_32x32x16_bf16 %2, v[156:159], v[100:103], %2\n\t" \
  "v_exp_f32 v124, v124\n\t" \
  "v_exp_f32 v125, v125\n\t" \
  "v_exp_f32 v126, v126\n\t" \
  "v_exp_f32 v127, v127\n\t" \
  "v_cvt_pk_bf16_f32 v116, v120, v121\n\t" \
  "v_cvt_pk_bf16_f32 v117, v122, v123\n\t" \
  "v_cvt_pk_bf16_f32 v118, v124, v125\n\t" \
  "v_cvt_pk_bf16_f32 v119, v126, v127\n\t" \
  "v_add_u32 %4, 18944, %4\n\t" \
  "v_add_u32 %5, 18944, %5\n\t" \
  "v_add_u32 %6, 18944, %6\n\t" \
  "s_add_u32 s40, s40, 1\n\t" \
  "s_cmp_lg_u32 s40, 3\n\t" \
  "s_cbranch_scc1 ATT_LOOPA_%=\n\t" \
  "s_waitcnt vmcnt(0)\n\t" \
  "s_barrier\n\t" \
  "ATT_LOOPB_%=:\n\t" \
  "v_mfma_f32_32x32x16_bf16 v[80:95], v[128:131], %8, 0\n\t" \
  "ds_read_b128 v[132:135], %4 offset:1024\n\t" \
  "ds_read_b32 v140, %5 offset:128\n\t" \
  "v_exp_f32 v64, v64\n\t" \
  "v_exp_f32 v65, v65\n\t" \
  "v_exp_f32 v66, v66\n\t" \
  "v_exp_f32 v67, v67\n\t" \
  "v_mfma_f32_32x32x16_bf16 v[80:95], v[136:139], %12, v[80:95]\n\t" \
  "v_exp_f32 v68, v68\n\t" \
  "v_exp_f32 v69, v69\n\t" \
  "v_exp_f32 v70, v70\n\t" \
  "v_exp_f32 v71, v71\n\t" \
  "v_cvt_pk_bf16_f32 v64, v64, v65\n\t" \
  "v_cvt_pk_bf16_f32 v65, v66, v67\n\t" \
  "v_mfma_f32_32x32x16_bf16 %3, v[152:155], v[112:115], %3\n\t" \
  "v_exp_f32 v72, v72\n\t" \
  "v_exp_f32 v73, v73\n\t" \
  "v_exp_f32 v74, v74\n\t" \
  "v_exp_f32 v75, v75\n\t" \
  "v_cvt_pk_bf16_f32 v66, v68, v69\n\t" \
  "v_cvt_pk_bf16_f32 v67, v70, v71\n\t" \
  "v_mfma_f32_32x32x16_bf16 %3, v[156:159], v[116:119], %3\n\t" \
  "v_exp_f32 v76, v76\n\t" \
  "v_exp_f32 v77, v77\n\t" \
  "v_exp_f32 v78, v78\n\t" \
  "v_exp_f32 v79, v79\n\t" \
  "v_cvt_pk_bf16_f32 v68, v72, v73\n\t" \
  "v_cvt_pk_bf16_f32 v69, v74, v75\n\t" \
  "v_cvt_pk_bf16_f32 v70, v76, v77\n\t" \
  "v_cvt_pk_bf16_f32 v71, v78, v79\n\t" \
  "v_mfma_f32_32x32x16_bf16 v[96:111], v[128:131], %9, 0\n\t" \
  "v_exp_f32 v80, v80\n\t" \
  "v_exp_f32 v81, v81\n\t" \
  "v_exp_f32 v82, v82\n\t" \
  "v_exp_f32 v83, v83\n\t" \
  "v_mfma_f32_32x32x16_bf16 v[96:111], v[136:139], %13, v[96:111]\n\t" \
  "v_exp_f32 v84, v84\n\t" \
  "v_exp_f32 v85, v85\n\t" \
  "v_exp_f32 v86, v86\n\t" \
  "v_exp_f32 v87, v87\n\t" \
  "v_cvt_pk_bf16_f32 v80, v80, v81\n\t" \
  "v_cvt_pk_bf16_f32 v81, v82, v83\n\t" \
  "s_waitcnt lgkmcnt(2)\n\t" \
  "v_mfma_f32_32x32x16_bf16 %0, v[144:147], v[64:67], %0\n\t" \
  "v_exp_f32 v88, v88\n\t" \
  "v_exp_f32 v89, v89\n\t" \
  "v_exp_f32 v90, v90\n\t" \
  "v_exp_f32 v91, v91\n\t" \
  "v_cvt_pk_bf16_f32 v82, v84, v85\n\t" \
  "v_cvt_pk_bf16_f32 v83, v86, v87\n\t" \
  "v_mfma_f32_32x32x16_bf16 %0, v[148:151], v[68:71], %0\n\t" \
  "v_exp_f32 v92, v92\n\t" \
  "v_exp_f32 v93, v93\n\t" \
  "v_exp_f32 v94, v94\n\t" \
  "v_exp_f32 v95, v95\n\t" \
  "v_cvt_pk_bf16_f32 v84, v88, v89\n\t" \
  "v_cvt_pk_bf16_f32 v85, v90, v91\n\t" \
  "v_cvt_pk_bf16_f32 v86, v92, v93\n\t" \
  "v_cvt_pk_bf16_f32 v87, v94, v95\n\t" \
  "v_mfma_f32_32x32x16_bf16 v[112:127], v[128:131], %10, 0\n\t" \
  "v_exp_f32 v96, v96\n\t" \
  "v_exp_f32 v97, v97\n\t" \
  "v_exp_f32 v98, v98\n\t" \
  "v_exp_f32 v99, v99\n\t" \
  "v_mfma_f32_32x32x16_bf16 v[112:127], v[136:139], %14, v[112:127]\n\t" \
  "ds_read_b128 v[152:155], %6 offset:1216\n\t" \
  "ds_read_b128 v[156:159], %6 offset:1824\n\t" \
  "v_exp_f32 v100, v100\n\t" \
  "v_exp_f32 v101, v101\n\t" \
  "v_exp_f32 v102, v102\n\t" \
  "v_exp_f32 v103, v103\n\t" \
  "v_cvt_pk_bf16_f32 v96, v96, v97\n\t" \
  "v_cvt_pk_bf16_f32 v97, v98, v99\n\t" \
  "v_mfma_f32_32x32x16_bf16 %1, v[144:147], v[80:83], %1\n\t" \
  "v_exp_f32 v104, v104\n\t" \
  "v_exp_f32 v105, v105\n\t" \
  "v_exp_f32 v106, v106\n\t" \
  "v_exp_f32 v107, v107\n\t" \
  "v_cvt_pk_bf16_f32 v98, v100, v101\n\t" \
  "v_cvt_pk_bf16_f32 v99, v102, v103\n\t" \
  "v_mfma_f32_32x32x16_bf16 %1, v[148:151], v[84:87], %1\n\t" \
  "v_exp_f32 v108, v108\n\t" \
  "v_exp_f32 v109, v109\n\t" \
  "v_exp_f32 v110, v110\n\t" \
  "v_exp_f32 v111, v111\n\t" \
  "v_cvt_pk_bf16_f32 v100, v104, v105\n\t" \
  "v_cvt_pk_bf16_f32 v101, v106, v107\n\t" \
  "v_cvt_pk_bf16_f32 v102, v108, v109\n\t" \
  "v_cvt_pk_bf16_f32 v103, v110, v111\n\t" \
  "s_waitcnt lgkmcnt(2)\n\t" \
  "v_mfma_f32_32x32x16_bf16 v[64:79], v[132:135], %7, 0\n\t" \
  "v_exp_f32 v112, v112\n\t" \
  "v_exp_f32 v113, v113\n\t" \
  "v_exp_f32 v114, v114\n\t" \
  "v_exp_f32 v115, v115\n\t" \
  "v_mfma_f32_32x32x16_bf16 v[64:79], v[140:143], %11, v[64:79]\n\t" \
  "v_exp_f32 v116, v116\n\t" \
  "v_exp_f32 v117, v117\n\t" \
  "v_exp_f32 v118, v118\n\t" \
  "v_exp_f32 v119, v119\n\t" \
  "v_cvt_pk_bf16_f32 v112, v112, v113\n\t" \
  "v_cvt_pk_bf16_f32 v113, v114, v115\n\t" \
  "v_mfma_f32_32x32x16_bf16 %2, v[144:147], v[96:99], %2\n\t" \
  "v_exp_f32 v120, v120\n\t" \
  "v_exp_f32 v121, v121\n\t" \
  "v_exp_f32 v122, v122\n\t" \
  "v_exp_f32 v123, v123\n\t" \
  "v_cvt_pk_bf16_f32 v114, v116, v117\n\t" \
  "v_cvt_pk_bf16_f32 v115, v118, v119\n\t" \
  "v_mfma_f32_32x32x16_bf16 %2, v[148:151], v[100:103], %2\n\t" \
  "v_exp_f32 v124, v124\n\t" \
  "v_exp_f32 v125, v125\n\t" \
  "v_exp_f32 v126, v126\n\t" \
  "v_exp_f32 v127, v127\n\t" \
  "v_cvt_pk_bf16_f32 v116, v120, v121\n\t" \
  "v_cvt_pk_bf16_f32 v117, v122, v123\n\t" \
  "v_cvt_pk_bf16_f32 v118, v124, v125\n\t" \
  "v_cvt_pk_bf16_f32 v119, v126, v127\n\t" \
  "v_mfma_f32_32x32x16_bf16 v[80:95], v[132:135], %8, 0\n\t" \
  "ds_read_b128 v[128:131], %4 offset:2048\n\t" \
  "ds_read_b32 v136, %5 offset:256\n\t" \
  "v_exp_f32 v64, v64\n\t" \
  "v_exp_f32 v65, v65\n\t" \
  "v_exp_f32 v66, v66\n\t" \
  "v_exp_f32 v67, v67\n\t" \
  "v_mfma_f32_32x32x16_bf16 v[80:95], v[140:143], %12, v[80:95]\n\t" \
  "v_exp_f32 v68, v68\n\t" \
  "v_exp_f32 v69, v69\n\t" \
  "v_exp_f32 v70, v70\n\t" \
  "v_exp_f32 v71, v71\n\t" \
  "v_cvt_pk_bf16_f32 v64, v64, v65\n\t" \
  "v_cvt_pk_bf16_f32 v65, v66, v67\n\t" \
  "v_mfma_f32_32x32x16_bf16 %3, v[144:147], v[112:115], %3\n\t" \
  "v_exp_f32 v72, v72\n\t" \
  "v_exp_f32 v73, v73\n\t" \
  "v_exp_f32 v74, v74\n\t" \
  "v_exp_f32 v75, v75\n\t" \
  "v_cvt_pk_bf16_f32 v66, v68, v69\n\t" \
  "v_cvt_pk_bf16_f32 v67, v70, v71\n\t" \
  "v_mfma_f32_32x32x16_bf16 %3, v[148:151], v[116:119], %3\n\t" \
  "v_exp_f32 v76, v76\n\t" \
  "v_exp_f32 v77, v77\n\t" \
  "v_exp_f32 v78, v78\n\t" \
  "v_exp_f32 v79, v79\n\t" \
  "v_cvt_pk_bf16_f32 v68, v72, v73\n\t" \
  "v_cvt_pk_bf16_f32 v69, v74, v75\n\t" \
  "v_cvt_pk_bf16_f32 v70, v76, v77\n\t" \
  "v_cvt_pk_bf16_f32 v71, v78, v79\n\t" \
  "v_mfma_f32_32x32x16_bf16 v[96:111], v[132:135], %9, 0\n\t" \
  "v_exp_f32 v80, v80\n\t" \
  "v_exp_f32 v81, v81\n\t" \
  "v_exp_f32 v82, v82\n\t" \
  "v_exp_f32 v83, v83\n\t" \
  "v_mfma_f32_32x32x16_bf16 v[96:111], v[140:143], %13, v[96:111]\n\t" \
  "v_exp_f32 v84, v84\n\t" \
  "v_exp_f32 v85, v85\n\t" \
  "v_exp_f32 v86, v86\n\t" \
  "v_exp_f32 v87, v87\n\t" \
  "v_cvt_pk_bf16_f32 v80, v80, v81\n\t" \
  "v_cvt_pk_bf16_f32 v81, v82, v83\n\t" \
  "s_waitcnt lgkmcnt(2)\n\t" \
  "v_mfma_f32_32x32x16_bf16 %0, v[152:155], v[64:67], %0\n\t" \
  "v_exp_f32 v88, v88\n\t" \
  "v_exp_f32 v89, v89\n\t" \
  "v_exp_f32 v90, v90\n\t" \
  "v_exp_f32 v91, v91\n\t" \
  "v_cvt_pk_bf16_f32 v82, v84, v85\n\t" \
  "v_cvt_pk_bf16_f32 v83, v86, v87\n\t" \
  "v_mfma_f32_32x32x16_bf16 %0, v[156:159], v[68:71], %0\n\t" \
  "v_exp_f32 v92, v92\n\t" \
  "v_exp_f32 v93, v93\n\t" \
  "v_exp_f32 v94, v94\n\t" \
  "v_exp_f32 v95, v95\n\t" \
  "v_cvt_pk_bf16_f32 v84, v88, v89\n\t" \
  "v_cvt_pk_bf16_f32 v85, v90, v91\n\t" \
  "v_cvt_pk_bf16_f32 v86, v92, v93\n\t" \
  "v_cvt_pk_bf16_f32 v87, v94, v95\n\t" \
  "v_mfma_f32_32x32x16_bf16 v[112:127], v[132:135], %10, 0\n\t" \
  "v_exp_f32 v96, v96\n\t" \
  "v_exp_f32 v97, v97\n\t" \
  "v_exp_f32 v98, v98\n\t" \
  "v_exp_f32 v99, v99\n\t" \
  "v_mfma_f32_32x32x16_bf16 v[112:127], v[140:143], %14, v[112:127]\n\t" \
  "ds_read_b128 v[144:147], %6 offset:2432\n\t" \
  "ds_read_b128 v[148:151], %6 offset:3040\n\t" \
  "v_exp_f32 v100, v100\n\t" \
  "v_exp_f32 v101, v101\n\t" \
  "v_exp_f32 v102, v102\n\t" \
  "v_exp_f32 v103, v103\n\t" \
  "v_cvt_pk_bf16_f32 v96, v96, v97\n\t" \
  "v_cvt_pk_bf16_f32 v97, v98, v99\n\t" \
  "v_mfma_f32_32x32x16_bf16 %1, v[152:155], v[80:83], %1\n\t" \
  "v_exp_f32 v104, v104\n\t" \
  "v_exp_f32 v105, v105\n\t" \
  "v_exp_f32 v106, v106\n\t" \
  "v_exp_f32 v107, v107\n\t" \
  "v_cvt_pk_bf16_f32 v98, v100, v101\n\t" \
  "v_cvt_pk_bf16_f32 v99, v102, v103\n\t" \
  "v_mfma_f32_32x32x16_bf16 %1, v[156:159], v[84:87], %1\n\t" \
  "v_exp_f32 v108, v108\n\t" \
  "v_exp_f32 v109, v109\n\t" \
  "v_exp_f32 v110, v110\n\t" \
  "v_exp_f32 v111, v111\n\t" \
  "v_cvt_pk_bf16_f32 v100, v104, v105\n\t" \
  "v_cvt_pk_bf16_f32 v101, v106, v107\n\t" \
  "v_cvt_pk_bf16_f32 v102, v108, v109\n\t" \
  "v_cvt_pk_bf16_f32 v103, v110, v111\n\t" \
  "s_waitcnt lgkmcnt(2)\n\t" \
  "v_mfma_f32_32x32x16_bf16 v[64:79], v[128:131], %7, 0\n\t" \
  "v_exp_f32 v112, v112\n\t" \
  "v_exp_f32 v113, v113\n\t" \
  "v_exp_f32 v114, v114\n\t" \
  "v_exp_f32 v115, v115\n\t" \
  "v_mfma_f32_32x32x16_bf16 v[64:79], v[136:139], %11, v[64:79]\n\t" \
  "v_exp_f32 v116, v116\n\t" \
  "v_exp_f32 v117, v117\n\t" \
  "v_exp_f32 v118, v118\n\t" \
  "v_exp_f32 v119, v119\n\t" \
  "v_cvt_pk_bf16_f32 v112, v112, v113\n\t" \
  "v_cvt_pk_bf16_f32 v113, v114, v115\n\t" \
  "v_mfma_f32_32x32x16_bf16 %2, v[152:155], v[96:99], %2\n\t" \
  "v_exp_f32 v120, v120\n\t" \
  "v_exp_f32 v121, v121\n\t" \
  "v_exp_f32 v122, v122\n\t" \
  "v_exp_f32 v123, v123\n\t" \
  "v_cvt_pk_bf16_f32 v114, v116, v117\n\t" \
  "v_cvt_pk_bf16_f32 v115, v118, v119\n\t" \
  "v_mfma_f32_32x32x16_bf16 %2, v[156:159], v[100:103], %2\n\t" \
  "v_exp_f32 v124, v124\n\t" \
  "v_exp_f32 v125, v125\n\t" \
  "v_exp_f32 v126, v126\n\t" \
  "v_exp_f32 v127, v127\n\t" \
  "v_cvt_pk_bf16_f32 v116, v120, v121\n\t" \
  "v_cvt_pk_bf16_f32 v117, v122, v123\n\t" \
  "v_cvt_pk_bf16_f32 v118, v124, v125\n\t" \
  "v_cvt_pk_bf16_f32 v119, v126, v127\n\t" \
  "v_mfma_f32_32x32x16_bf16 v[80:95], v[128:131], %8, 0\n\t" \
  "ds_read_b128 v[132:135], %4 offset:3072\n\t" \
  "ds_read_b32 v140, %5 offset:384\n\t" \
  "v_exp_f32 v64, v64\n\t" \
  "v_exp_f32 v65, v65\n\t" \
  "v_exp_f32 v66, v66\n\t" \
  "v_exp_f32 v67, v67\n\t" \
  "v_mfma_f32_32x32x16_bf16 v[80:95], v[136:139], %12, v[80:95]\n\t" \
  "v_exp_f32 v68, v68\n\t" \
  "v_exp_f32 v69, v69\n\t" \
  "v_exp_f32 v70, v70\n\t" \
  "v_exp_f32 v71, v71\n\t" \
  "v_cvt_pk_bf16_f32 v64, v64, v65\n\t" \
  "v_cvt_pk_bf16_f32 v65, v66, v67\n\t" \
  "v_mfma_f32_32x32x16_bf16 %3, v[152:155], v[112:115], %3\n\t" \
  "v_exp_f32 v72, v72\n\t" \
  "v_exp_f32 v73, v73\n\t" \
  "v_exp_f32 v74, v74\n\t" \
  "v_exp_f32 v75, v75\n\t" \
  "v_cvt_pk_bf16_f32 v66, v68, v69\n\t" \
  "v_cvt_pk_bf16_f32 v67, v70, v71\n\t" \
  "v_mfma_f32_32x32x16_bf16 %3, v[156:159], v[116:119], %3\n\t" \
  "v_exp_f32 v76, v76\n\t" \
  "v_exp_f32 v77, v77\n\t" \
  "v_exp_f32 v78, v78\n\t" \
  "v_exp_f32 v79, v79\n\t" \
  "v_cvt_pk_bf16_f32 v68, v72, v73\n\t" \
  "v_cvt_pk_bf16_f32 v69, v74, v75\n\t" \
  "v_cvt_pk_bf16_f32 v70, v76, v77\n\t" \
  "v_cvt_pk_bf16_f32 v71, v78, v79\n\t" \
  "v_mfma_f32_32x32x16_bf16 v[96:111], v[128:131], %9, 0\n\t" \
  "v_exp_f32 v80, v80\n\t" \
  "v_exp_f32 v81, v81\n\t" \
  "v_exp_f32 v82, v82\n\t" \
  "v_exp_f32 v83, v83\n\t" \
  "v_mfma_f32_32x32x16_bf16 v[96:111], v[136:139], %13, v[96:111]\n\t" \
  "v_exp_f32 v84, v84\n\t" \
  "v_exp_f32 v85, v85\n\t" \
  "v_exp_f32 v86, v86\n\t" \
  "v_exp_f32 v87, v87\n\t" \
  "v_cvt_pk_bf16_f32 v80, v80, v81\n\t" \
  "v_cvt_pk_bf16_f32 v81, v82, v83\n\t" \
  "s_waitcnt lgkmcnt(2)\n\t" \
  "v_mfma_f32_32x32x16_bf16 %0, v[144:147], v[64:67], %0\n\t" \
  "v_exp_f32 v88, v88\n\t" \
  "v_exp_f32 v89, v89\n\t" \
  "v_exp_f32 v90, v90\n\t" \
  "v_exp_f32 v91, v91\n\t" \
  "v_cvt_pk_bf16_f32 v82, v84, v85\n\t" \
  "v_cvt_pk_bf16_f32 v83, v86, v87\n\t" \
  "v_mfma_f32_32x32x16_bf16 %0, v[148:151], v[68:71], %0\n\t" \
  "v_exp_f32 v92, v92\n\t" \
  "v_exp_f32 v93, v93\n\t" \
  "v_exp_f32 v94, v94\n\t" \
  "v_exp_f32 v95, v95\n\t" \
  "v_cvt_pk_bf16_f32 v84, v88, v89\n\t" \
  "v_cvt_pk_bf16_f32 v85, v90, v91\n\t" \
  "v_cvt_pk_bf16_f32 v86, v92, v93\n\t" \
  "v_cvt_pk_bf16_f32 v87, v94, v95\n\t" \
  "v_mfma_f32_32x32x16_bf16 v[112:127], v[128:131], %10, 0\n\t" \
  "v_exp_f32 v96, v96\n\t" \
  "v_exp_f32 v97, v97\n\t" \
  "v_exp_f32 v98, v98\n\t" \
  "v_exp_f32 v99, v99\n\t" \
  "v_mfma_f32_32x32x16_bf16 v[112:127], v[136:139], %14, v[112:127]\n\t" \
  "ds_read_b128 v[152:155], %6 offset:3648\n\t" \
  "ds_read_b128 v[156:159], %6 offset:4256\n\t" \
  "v_exp_f32 v100, v100\n\t" \
  "v_exp_f32 v101, v101\n\t" \
  "v_exp_f32 v102, v102\n\t" \
  "v_exp_f32 v103, v103\n\t" \
  "v_cvt_pk_bf16_f32 v96, v96, v97\n\t" \
  "v_cvt_pk_bf16_f32 v97, v98, v99\n\t" \
  "v_mfma_f32_32x32x16_bf16 %1, v[144:147], v[80:83], %1\n\t" \
  "v_exp_f32 v104, v104\n\t" \
  "v_exp_f32 v105, v105\n\t" \
  "v_exp_f32 v106, v106\n\t" \
  "v_exp_f32 v107, v107\n\t" \
  "v_cvt_pk_bf16_f32 v98, v100, v101\n\t" \
  "v_cvt_pk_bf16_f32 v99, v102, v103\n\t" \
  "v_mfma_f32_32x32x16_bf16 %1, v[148:151], v[84:87], %1\n\t" \
  "v_exp_f32 v108, v108\n\t" \
  "v_exp_f32 v109, v109\n\t" \
  "v_exp_f32 v110, v110\n\t" \
  "v_exp_f32 v111, v111\n\t" \
  "v_cvt_pk_bf16_f32 v100, v104, v105\n\t" \
  "v_cvt_pk_bf16_f32 v101, v106, v107\n\t" \
  "v_cvt_pk_bf16_f32 v102, v108, v109\n\t" \
  "v_cvt_pk_bf16_f32 v103, v110, v111\n\t" \
  "s_waitcnt lgkmcnt(2)\n\t" \
  "v_mfma_f32_32x32x16_bf16 v[64:79], v[132:135], %7, 0\n\t" \
  "v_exp_f32 v112, v112\n\t" \
  "v_exp_f32 v113, v113\n\t" \
  "v_exp_f32 v114, v114\n\t" \
  "v_exp_f32 v115, v115\n\t" \
  "v_mfma_f32_32x32x16_bf16 v[64:79], v[140:143], %11, v[64:79]\n\t" \
  "v_exp_f32 v116, v116\n\t" \
  "v_exp_f32 v117, v117\n\t" \
  "v_exp_f32 v118, v118\n\t" \
  "v_exp_f32 v119, v119\n\t" \
  "v_cvt_pk_bf16_f32 v112, v112, v113\n\t" \
  "v_cvt_pk_bf16_f32 v113, v114, v115\n\t" \
  "v_mfma_f32_32x32x16_bf16 %2, v[144:147], v[96:99], %2\n\t" \
  "v_exp_f32 v120, v120\n\t" \
  "v_exp_f32 v121, v121\n\t" \
  "v_exp_f32 v122, v122\n\t" \
  "v_exp_f32 v123, v123\n\t" \
  "v_cvt_pk_bf16_f32 v114, v116, v117\n\t" \
  "v_cvt_pk_bf16_f32 v115, v118, v119\n\t" \
  "v_mfma_f32_32x32x16_bf16 %2, v[148:151], v[100:103], %2\n\t" \
  "v_exp_f32 v124, v124\n\t" \
  "v_exp_f32 v125, v125\n\t" \
  "v_exp_f32 v126, v126\n\t" \
  "v_exp_f32 v127, v127\n\t" \
  "v_cvt_pk_bf16_f32 v116, v120, v121\n\t" \
  "v_cvt_pk_bf16_f32 v117, v122, v123\n\t" \
  "v_cvt_pk_bf16_f32 v118, v124, v125\n\t" \
  "v_cvt_pk_bf16_f32 v119, v126, v127\n\t" \
  "v_mfma_f32_32x32x16_bf16 v[80:95], v[132:135], %8, 0\n\t" \
  "ds_read_b128 v[128:131], %4 offset:4096\n\t" \
  "ds_read_b32 v136, %5 offset:512\n\t" \
  "v_exp_f32 v64, v64\n\t" \
  "v_exp_f32 v65, v65\n\t" \
  "v_exp_f32 v66, v66\n\t" \
  "v_exp_f32 v67, v67\n\t" \
  "v_mfma_f32_32x32x16_bf16 v[80:95], v[140:143], %12, v[80:95]\n\t" \
  "v_exp_f32 v68, v68\n\t" \
  "v_exp_f32 v69, v69\n\t" \
  "v_exp_f32 v70, v70\n\t" \
  "v_exp_f32 v71, v71\n\t" \
  "v_cvt_pk_bf16_f32 v64, v64, v65\n\t" \
  "v_cvt_pk_bf16_f32 v65, v66, v67\n\t" \
  "v_mfma_f32_32x32x16_bf16 %3, v[144:147], v[112:115], %3\n\t" \
  "v_exp_f32 v72, v72\n\t" \
  "v_exp_f32 v73, v73\n\t" \
  "v_exp_f32 v74, v74\n\t" \
  "v_exp_f32 v75, v75\n\t" \
  "v_cvt_pk_bf16_f32 v66, v68, v69\n\t" \
  "v_cvt_pk_bf16_f32 v67, v70, v71\n\t" \
  "v_mfma_f32_32x32x16_bf16 %3, v[148:151], v[116:119], %3\n\t" \
  "v_exp_f32 v76, v76\n\t" \
  "v_exp_f32 v77, v77\n\t" \
  "v_exp_f32 v78, v78\n\t" \
  "v_exp_f32 v79, v79\n\t" \
  "v_cvt_pk_bf16_f32 v68, v72, v73\n\t" \
  "v_cvt_pk_bf16_f32 v69, v74, v75\n\t" \
  "v_cvt_pk_bf16_f32 v70, v76, v77\n\t" \
  "v_cvt_pk_bf16_f32 v71, v78, v79\n\t" \
  "v_mfma_f32_32x32x16_bf16 v[96:111], v[132:135], %9, 0\n\t" \
  "v_exp_f32 v80, v80\n\t" \
  "v_exp_f32 v81, v81\n\t" \
  "v_exp_f32 v82, v82\n\t" \
  "v_exp_f32 v83, v83\n\t" \
  "v_mfma_f32_32x32x16_bf16 v[96:111], v[140:143], %13, v[96:111]\n\t" \
  "v_exp_f32 v84, v84\n\t" \
  "v_exp_f32 v85, v85\n\t" \
  "v_exp_f32 v86, v86\n\t" \
  "v_exp_f32 v87, v87\n\t" \
  "v_cvt_pk_bf16_f32 v80, v80, v81\n\t" \
  "v_cvt_pk_bf16_f32 v81, v82, v83\n\t" \
  "s_waitcnt lgkmcnt(2)\n\t" \
  "v_mfma_f32_32x32x16_bf16 %0, v[152:155], v[64:67], %0\n\t" \
  "v_exp_f32 v88, v88\n\t" \
  "v_exp_f32 v89, v89\n\t" \
  "v_exp_f32 v90, v90\n\t" \
  "v_exp_f32 v91, v91\n\t" \
  "v_cvt_pk_bf16_f32 v82, v84, v85\n\t" \
  "v_cvt_pk_bf16_f32 v83, v86, v87\n\t" \
  "v_mfma_f32_32x32x16_bf16 %0, v[156:159], v[68:71], %0\n\t" \
  "v_exp_f32 v92, v92\n\t" \
  "v_exp_f32 v93, v93\n\t" \
  "v_exp_f32 v94, v94\n\t" \
  "v_exp_f32 v95, v95\n\t" \
  "v_cvt_pk_bf16_f32 v84, v88, v89\n\t" \
  "v_cvt_pk_bf16_f32 v85, v90, v91\n\t" \
  "v_cvt_pk_bf16_f32 v86, v92, v93\n\t" \
  "v_cvt_pk_bf16_f32 v87, v94, v95\n\t" \
  "v_mfma_f32_32x32x16_bf16 v[112:127], v[132:135], %10, 0\n\t" \
  "v_exp_f32 v96, v96\n\t" \
  "v_exp_f32 v97, v97\n\t" \
  "v_exp_f32 v98, v98\n\t" \
  "v_exp_f32 v99, v99\n\t" \
  "v_mfma_f32_32x32x16_bf16 v[112:127], v[140:143], %14, v[112:127]\n\t" \
  "ds_read_b128 v[144:147], %6 offset:4864\n\t" \
  "ds_read_b128 v[148:151], %6 offset:5472\n\t" \
  "v_exp_f32 v100, v100\n\t" \
  "v_exp_f32 v101, v101\n\t" \
  "v_exp_f32 v102, v102\n\t" \
  "v_exp_f32 v103, v103\n\t" \
  "v_cvt_pk_bf16_f32 v96, v96, v97\n\t" \
  "v_cvt_pk_bf16_f32 v97, v98, v99\n\t" \
  "v_mfma_f32_32x32x16_bf16 %1, v[152:155], v[80:83], %1\n\t" \
  "v_exp_f32 v104, v104\n\t" \
  "v_exp_f32 v105, v105\n\t" \
  "v_exp_f32 v106, v106\n\t" \
  "v_exp_f32 v107, v107\n\t" \
  "v_cvt_pk_bf16_f32 v98, v100, v101\n\t" \
  "v_cvt_pk_bf16_f32 v99, v102, v103\n\t" \
  "v_mfma_f32_32x32x16_bf16 %1, v[156:159], v[84:87], %1\n\t" \
  "v_exp_f32 v108, v108\n\t" \
  "v_exp_f32 v109, v109\n\t" \
  "v_exp_f32 v110, v110\n\t" \
  "v_exp_f32 v111, v111\n\t" \
  "v_cvt_pk_bf16_f32 v100, v104, v105\n\t" \
  "v_cvt_pk_bf16_f32 v101, v106, v107\n\t" \
  "v_cvt_pk_bf16_f32 v102, v108, v109\n\t" \
  "v_cvt_pk_bf16_f32 v103, v110, v111\n\t" \
  "s_waitcnt lgkmcnt(2)\n\t" \
  "v_mfma_f32_32x32x16_bf16 v[64:79], v[128:131], %7, 0\n\t" \
  "v_exp_f32 v112, v112\n\t" \
  "v_exp_f32 v113, v113\n\t" \
  "v_exp_f32 v114, v114\n\t" \
  "v_exp_f32 v115, v115\n\t" \
  "v_mfma_f32_32x32x16_bf16 v[64:79], v[136:139], %11, v[64:79]\n\t" \
  "v_exp_f32 v116, v116\n\t" \
  "v_exp_f32 v117, v117\n\t" \
  "v_exp_f32 v118, v118\n\t" \
  "v_exp_f32 v119, v119\n\t" \
  "v_cvt_pk_bf16_f32 v112, v112, v113\n\t" \
  "v_cvt_pk_bf16_f32 v113, v114, v115\n\t" \
  "v_mfma_f32_32x32x16_bf16 %2, v[152:155], v[96:99], %2\n\t" \
  "v_exp_f32 v120, v120\n\t" \
  "v_exp_f32 v121, v121\n\t" \
  "v_exp_f32 v122, v122\n\t" \
  "v_exp_f32 v123, v123\n\t" \
  "v_cvt_pk_bf16_f32 v114, v116, v117\n\t" \
  "v_cvt_pk_bf16_f32 v115, v118, v119\n\t" \
  "v_mfma_f32_32x32x16_bf16 %2, v[156:159], v[100:103], %2\n\t" \
  "v_exp_f32 v124, v124\n\t" \
  "v_exp_f32 v125, v125\n\t" \
  "v_exp_f32 v126, v126\n\t" \
  "v_exp_f32 v127, v127\n\t" \
  "v_cvt_pk_bf16_f32 v116, v120, v121\n\t" \
  "v_cvt_pk_bf16_f32 v117, v122, v123\n\t" \
  "v_cvt_pk_bf16_f32 v118, v124, v125\n\t" \
  "v_cvt_pk_bf16_f32 v119, v126, v127\n\t" \
  "v_mfma_f32_32x32x16_bf16 v[80:95], v[128:131], %8, 0\n\t" \
  "ds_read_b128 v[132:135], %4 offset:5120\n\t" \
  "ds_read_b32 v140, %5 offset:640\n\t" \
  "v_exp_f32 v64, v64\n\t" \
  "v_exp_f32 v65, v65\n\t" \
  "v_exp_f32 v66, v66\n\t" \
  "v_exp_f32 v67, v67\n\t" \
  "v_mfma_f32_32x32x16_bf16 v[80:95], v[136:139], %12, v[80:95]\n\t" \
  "v_exp_f32 v68, v68\n\t" \
  "v_exp_f32 v69, v69\n\t" \
  "v_exp_f32 v70, v70\n\t" \
  "v_exp_f32 v71, v71\n\t" \
  "v_cvt_pk_bf16_f32 v64, v64, v65\n\t" \
  "v_cvt_pk_bf16_f32 v65, v66, v67\n\t" \
  "v_mfma_f32_32x32x16_bf16 %3, v[152:155], v[112:115], %3\n\t" \
  "v_exp_f32 v72, v72\n\t" \
  "v_exp_f32 v73, v73\n\t" \
  "v_exp_f32 v74, v74\n\t" \
  "v_exp_f32 v75, v75\n\t" \
  "v_cvt_pk_bf16_f32 v66, v68, v69\n\t" \
  "v_cvt_pk_bf16_f32 v67, v70, v71\n\t" \
  "v_mfma_f32_32x32x16_bf16 %3, v[156:159], v[116:119], %3\n\t" \
  "v_exp_f32 v76, v76\n\t" \
  "v_exp_f32 v77, v77\n\t" \
  "v_exp_f32 v78, v78\n\t" \
  "v_exp_f32 v79, v79\n\t" \
  "v_cvt_pk_bf16_f32 v68, v72, v73\n\t" \
  "v_cvt_pk_bf16_f32 v69, v74, v75\n\t" \
  "v_cvt_pk_bf16_f32 v70, v76, v77\n\t" \
  "v_cvt_pk_bf16_f32 v71, v78, v79\n\t" \
  "v_mfma_f32_32x32x16_bf16 v[96:111], v[128:131], %9, 0\n\t" \
  "v_exp_f32 v80, v80\n\t" \
  "v_exp_f32 v81, v81\n\t" \
  "v_exp_f32 v82, v82\n\t" \
  "v_exp_f32 v83, v83\n\t" \
  "v_mfma_f32_32x32x16_bf16 v[96:111], v[136:139], %13, v[96:111]\n\t" \
  "v_exp_f32 v84, v84\n\t" \
  "v_exp_f32 v85, v85\n\t" \
  "v_exp_f32 v86, v86\n\t" \
  "v_exp_f32 v87, v87\n\t" \
  "v_cvt_pk_bf16_f32 v80, v80, v81\n\t" \
  "v_cvt_pk_bf16_f32 v81, v82, v83\n\t" \
  "s_waitcnt lgkmcnt(2)\n\t" \
  "v_mfma_f32_32x32x16_bf16 %0, v[144:147], v[64:67], %0\n\t" \
  "v_exp_f32 v88, v88\n\t" \
  "v_exp_f32 v89, v89\n\t" \
  "v_exp_f32 v90, v90\n\t" \
  "v_exp_f32 v91, v91\n\t" \
  "v_cvt_pk_bf16_f32 v82, v84, v85\n\t" \
  "v_cvt_pk_bf16_f32 v83, v86, v87\n\t" \
  "v_mfma_f32_32x32x16_bf16 %0, v[148:151], v[68:71], %0\n\t" \
  "v_exp_f32 v92, v92\n\t" \
  "v_exp_f32 v93, v93\n\t" \
  "v_exp_f32 v94, v94\n\t" \
  "v_exp_f32 v95, v95\n\t" \
  "v_cvt_pk_bf16_f32 v84, v88, v89\n\t" \
  "v_cvt_pk_bf16_f32 v85, v90, v91\n\t" \
  "v_cvt_pk_bf16_f32 v86, v92, v93\n\t" \
  "v_cvt_pk_bf16_f32 v87, v94, v95\n\t" \
  "v_mfma_f32_32x32x16_bf16 v[112:127], v[128:131], %10, 0\n\t" \
  "v_exp_f32 v96, v96\n\t" \
  "v_exp_f32 v97, v97\n\t" \
  "v_exp_f32 v98, v98\n\t" \
  "v_exp_f32 v99, v99\n\t" \
  "v_mfma_f32_32x32x16_bf16 v[112:127], v[136:139], %14, v[112:127]\n\t" \
  "ds_read_b128 v[152:155], %6 offset:6080\n\t" \
  "ds_read_b128 v[156:159], %6 offset:6688\n\t" \
  "v_exp_f32 v100, v100\n\t" \
  "v_exp_f32 v101, v101\n\t" \
  "v_exp_f32 v102, v102\n\t" \
  "v_exp_f32 v103, v103\n\t" \
  "v_cvt_pk_bf16_f32 v96, v96, v97\n\t" \
  "v_cvt_pk_bf16_f32 v97, v98, v99\n\t" \
  "v_mfma_f32_32x32x16_bf16 %1, v[144:147], v[80:83], %1\n\t" \
  "v_exp_f32 v104, v104\n\t" \
  "v_exp_f32 v105, v105\n\t" \
  "v_exp_f32 v106, v106\n\t" \
  "v_exp_f32 v107, v107\n\t" \
  "v_cvt_pk_bf16_f32 v98, v100, v101\n\t" \
  "v_cvt_pk_bf16_f32 v99, v102, v103\n\t" \
  "v_mfma_f32_32x32x16_bf16 %1, v[148:151], v[84:87], %1\n\t" \
  "v_exp_f32 v108, v108\n\t" \
  "v_exp_f32 v109, v109\n\t" \
  "v_exp_f32 v110, v110\n\t" \
  "v_exp_f32 v111, v111\n\t" \
  "v_cvt_pk_bf16_f32 v100, v104, v105\n\t" \
  "v_cvt_pk_bf16_f32 v101, v106, v107\n\t" \
  "v_cvt_pk_bf16_f32 v102, v108, v109\n\t" \
  "v_cvt_pk_bf16_f32 v103, v110, v111\n\t" \
  "s_waitcnt lgkmcnt(2)\n\t" \
  "v_mfma_f32_32x32x16_bf16 v[64:79], v[132:135], %7, 0\n\t" \
  "v_exp_f32 v112, v112\n\t" \
  "v_exp_f32 v113, v113\n\t" \
  "v_exp_f32 v114, v114\n\t" \
  "v_exp_f32 v115, v115\n\t" \
  "v_mfma_f32_32x32x16_bf16 v[64:79], v[140:143], %11, v[64:79]\n\t" \
  "v_exp_f32 v116, v116\n\t" \
  "v_exp_f32 v117, v117\n\t" \
  "v_exp_f32 v118, v118\n\t" \
  "v_exp_f32 v119, v119\n\t" \
  "v_cvt_pk_bf16_f32 v112, v112, v113\n\t" \
  "v_cvt_pk_bf16_f32 v113, v114, v115\n\t" \
  "v_mfma_f32_32x32x16_bf16 %2, v[144:147], v[96:99], %2\n\t" \
  "v_exp_f32 v120, v120\n\t" \
  "v_exp_f32 v121, v121\n\t" \
  "v_exp_f32 v122, v122\n\t" \
  "v_exp_f32 v123, v123\n\t" \
  "v_cvt_pk_bf16_f32 v114, v116, v117\n\t" \
  "v_cvt_pk_bf16_f32 v115, v118, v119\n\t" \
  "v_mfma_f32_32x32x16_bf16 %2, v[148:151], v[100:103], %2\n\t" \
  "v_exp_f32 v124, v124\n\t" \
  "v_exp_f32 v125, v125\n\t" \
  "v_exp_f32 v126, v126\n\t" \
  "v_exp_f32 v127, v127\n\t" \
  "v_cvt_pk_bf16_f32 v116, v120, v121\n\t" \
  "v_cvt_pk_bf16_f32 v117, v122, v123\n\t" \
  "v_cvt_pk_bf16_f32 v118, v124, v125\n\t" \
  "v_cvt_pk_bf16_f32 v119, v126, v127\n\t" \
  "v_mfma_f32_32x32x16_bf16 v[80:95], v[132:135], %8, 0\n\t" \
  "ds_read_b128 v[128:131], %4 offset:6144\n\t" \
  "ds_read_b32 v136, %5 offset:768\n\t" \
  "v_exp_f32 v64, v64\n\t" \
  "v_exp_f32 v65, v65\n\t" \
  "v_exp_f32 v66, v66\n\t" \
  "v_exp_f32 v67, v67\n\t" \
  "v_mfma_f32_32x32x16_bf16 v[80:95], v[140:143], %12, v[80:95]\n\t" \
  "v_exp_f32 v68, v68\n\t" \
  "v_exp_f32 v69, v69\n\t" \
  "v_exp_f32 v70, v70\n\t" \
  "v_exp_f32 v71, v71\n\t" \
  "v_cvt_pk_bf16_f32 v64, v64, v65\n\t" \
  "v_cvt_pk_bf16_f32 v65, v66, v67\n\t" \
  "v_mfma_f32_32x32x16_bf16 %3, v[144:147], v[112:115], %3\n\t" \
  "v_exp_f32 v72, v72\n\t" \
  "v_exp_f32 v73, v73\n\t" \
  "v_exp_f32 v74, v74\n\t" \
  "v_exp_f32 v75, v75\n\t" \
  "v_cvt_pk_bf16_f32 v66, v68, v69\n\t" \
  "v_cvt_pk_bf16_f32 v67, v70, v71\n\t" \
  "v_mfma_f32_32x32x16_bf16 %3, v[148:151], v[116:119], %3\n\t" \
  "v_exp_f32 v76, v76\n\t" \
  "v_exp_f32 v77, v77\n\t" \
  "v_exp_f32 v78, v78\n\t" \
  "v_exp_f32 v79, v79\n\t" \
  "v_cvt_pk_bf16_f32 v68, v72, v73\n\t" \
  "v_cvt_pk_bf16_f32 v69, v74, v75\n\t" \
  "v_cvt_pk_bf16_f32 v70, v76, v77\n\t" \
  "v_cvt_pk_bf16_f32 v71, v78, v79\n\t" \
  "v_mfma_f32_32x32x16_bf16 v[96:111], v[132:135], %9, 0\n\t" \
  "v_exp_f32 v80, v80\n\t" \
  "v_exp_f32 v81, v81\n\t" \
  "v_exp_f32 v82, v82\n\t" \
  "v_exp_f32 v83, v83\n\t" \
  "v_mfma_f32_32x32x16_bf16 v[96:111], v[140:143], %13, v[96:111]\n\t" \
  "v_exp_f32 v84, v84\n\t" \
  "v_exp_f32 v85, v85\n\t" \
  "v_exp_f32 v86, v86\n\t" \
  "v_exp_f32 v87, v87\n\t" \
  "v_cvt_pk_bf16_f32 v80, v80, v81\n\t" \
  "v_cvt_pk_bf16_f32 v81, v82, v83\n\t" \
  "s_waitcnt lgkmcnt(2)\n\t" \
  "v_mfma_f32_32x32x16_bf16 %0, v[152:155], v[64:67], %0\n\t" \
  "v_exp_f32 v88, v88\n\t" \
  "v_exp_f32 v89, v89\n\t" \
  "v_exp_f32 v90, v90\n\t" \
  "v_exp_f32 v91, v91\n\t" \
  "v_cvt_pk_bf16_f32 v82, v84, v85\n\t" \
  "v_cvt_pk_bf16_f32 v83, v86, v87\n\t" \
  "v_mfma_f32_32x32x16_bf16 %0, v[156:159], v[68:71], %0\n\t" \
  "v_exp_f32 v92, v92\n\t" \
  "v_exp_f32 v93, v93\n\t" \
  "v_exp_f32 v94, v94\n\t" \
  "v_exp_f32 v95, v95\n\t" \
  "v_cvt_pk_bf16_f32 v84, v88, v89\n\t" \
  "v_cvt_pk_bf16_f32 v85, v90, v91\n\t" \
  "v_cvt_pk_bf16_f32 v86, v92, v93\n\t" \
  "v_cvt_pk_bf16_f32 v87, v94, v95\n\t" \
  "v_mfma_f32_32x32x16_bf16 v[112:127], v[132:135], %10, 0\n\t" \
  "v_exp_f32 v96, v96\n\t" \
  "v_exp_f32 v97, v97\n\t" \
  "v_exp_f32 v98, v98\n\t" \
  "v_exp_f32 v99, v99\n\t" \
  "v_mfma_f32_32x32x16_bf16 v[112:127], v[140:143], %14, v[112:127]\n\t" \
  "ds_read_b128 v[144:147], %6 offset:7296\n\t" \
  "ds_read_b128 v[148:151], %6 offset:7904\n\t" \
  "v_exp_f32 v100, v100\n\t" \
  "v_exp_f32 v101, v101\n\t" \
  "v_exp_f32 v102, v102\n\t" \
  "v_exp_f32 v103, v103\n\t" \
  "v_cvt_pk_bf16_f32 v96, v96, v97\n\t" \
  "v_cvt_pk_bf16_f32 v97, v98, v99\n\t" \
  "v_mfma_f32_32x32x16_bf16 %1, v[152:155], v[80:83], %1\n\t" \
  "v_exp_f32 v104, v104\n\t" \
  "v_exp_f32 v105, v105\n\t" \
  "v_exp_f32 v106, v106\n\t" \
  "v_exp_f32 v107, v107\n\t" \
  "v_cvt_pk_bf16_f32 v98, v100, v101\n\t" \
  "v_cvt_pk_bf16_f32 v99, v102, v103\n\t" \
  "v_mfma_f32_32x32x16_bf16 %1, v[156:159], v[84:87], %1\n\t" \
  "v_exp_f32 v108, v108\n\t" \
  "v_exp_f32 v109, v109\n\t" \
  "v_exp_f32 v110, v110\n\t" \
  "v_exp_f32 v111, v111\n\t" \
  "v_cvt_pk_bf16_f32 v100, v104, v105\n\t" \
  "v_cvt_pk_bf16_f32 v101, v106, v107\n\t" \
  "v_cvt_pk_bf16_f32 v102, v108, v109\n\t" \
  "v_cvt_pk_bf16_f32 v103, v110, v111\n\t" \
  "s_waitcnt lgkmcnt(2)\n\t" \
  "v_mfma_f32_32x32x16_bf16 v[64:79], v[128:131], %7, 0\n\t" \
  "v_exp_f32 v112, v112\n\t" \
  "v_exp_f32 v113, v113\n\t" \
  "v_exp_f32 v114, v114\n\t" \
  "v_exp_f32 v115, v115\n\t" \
  "v_mfma_f32_32x32x16_bf16 v[64:79], v[136:139], %11, v[64:79]\n\t" \
  "v_exp_f32 v116, v116\n\t" \
  "v_exp_f32 v117, v117\n\t" \
  "v_exp_f32 v118, v118\n\t" \
  "v_exp_f32 v119, v119\n\t" \
  "v_cvt_pk_bf16_f32 v112, v112, v113\n\t" \
  "v_cvt_pk_bf16_f32 v113, v114, v115\n\t" \
  "v_mfma_f32_32x32x16_bf16 %2, v[152:155], v[96:99], %2\n\t" \
  "v_exp_f32 v120, v120\n\t" \
  "v_exp_f32 v121, v121\n\t" \
  "v_exp_f32 v122, v122\n\t" \
  "v_exp_f32 v123, v123\n\t" \
  "v_cvt_pk_bf16_f32 v114, v116, v117\n\t" \
  "v_cvt_pk_bf16_f32 v115, v118, v119\n\t" \
  "v_mfma_f32_32x32x16_bf16 %2, v[156:159], v[100:103], %2\n\t" \
  "v_exp_f32 v124, v124\n\t" \
  "v_exp_f32 v125, v125\n\t" \
  "v_exp_f32 v126, v126\n\t" \
  "v_exp_f32 v127, v127\n\t" \
  "v_cvt_pk_bf16_f32 v116, v120, v121\n\t" \
  "v_cvt_pk_bf16_f32 v117, v122, v123\n\t" \
  "v_cvt_pk_bf16_f32 v118, v124, v125\n\t" \
  "v_cvt_pk_bf16_f32 v119, v126, v127\n\t" \
  "v_mfma_f32_32x32x16_bf16 v[80:95], v[128:131], %8, 0\n\t" \
  "ds_read_b128 v[132:135], %4 offset:7168\n\t" \
  "ds_read_b32 v140, %5 offset:896\n\t" \
  "v_exp_f32 v64, v64\n\t" \
  "v_exp_f32 v65, v65\n\t" \
  "v_exp_f32 v66, v66\n\t" \
  "v_exp_f32 v67, v67\n\t" \
  "v_mfma_f32_32x32x16_bf16 v[80:95], v[136:139], %12, v[80:95]\n\t" \
  "v_exp_f32 v68, v68\n\t" \
  "v_exp_f32 v69, v69\n\t" \
  "v_exp_f32 v70, v70\n\t" \
  "v_exp_f32 v71, v71\n\t" \
  "v_cvt_pk_bf16_f32 v64, v64, v65\n\t" \
  "v_cvt_pk_bf16_f32 v65, v66, v67\n\t" \
  "v_mfma_f32_32x32x16_bf16 %3, v[152:155], v[112:115], %3\n\t" \
  "v_exp_f32 v72, v72\n\t" \
  "v_exp_f32 v73, v73\n\t" \
  "v_exp_f32 v74, v74\n\t" \
  "v_exp_f32 v75, v75\n\t" \
  "v_cvt_pk_bf16_f32 v66, v68, v69\n\t" \
  "v_cvt_pk_bf16_f32 v67, v70, v71\n\t" \
  "v_mfma_f32_32x32x16_bf16 %3, v[156:159], v[116:119], %3\n\t" \
  "v_exp_f32 v76, v76\n\t" \
  "v_exp_f32 v77, v77\n\t" \
  "v_exp_f32 v78, v78\n\t" \
  "v_exp_f32 v79, v79\n\t" \
  "v_cvt_pk_bf16_f32 v68, v72, v73\n\t" \
  "v_cvt_pk_bf16_f32 v69, v74, v75\n\t" \
  "v_cvt_pk_bf16_f32 v70, v76, v77\n\t" \
  "v_cvt_pk_bf16_f32 v71, v78, v79\n\t" \
  "v_mfma_f32_32x32x16_bf16 v[96:111], v[128:131], %9, 0\n\t" \
  "v_exp_f32 v80, v80\n\t" \
  "v_exp_f32 v81, v81\n\t" \
  "v_exp_f32 v82, v82\n\t" \
  "v_exp_f32 v83, v83\n\t" \
  "v_mfma_f32_32x32x16_bf16 v[96:111], v[136:139], %13, v[96:111]\n\t" \
  "v_exp_f32 v84, v84\n\t" \
  "v_exp_f32 v85, v85\n\t" \
  "v_exp_f32 v86, v86\n\t" \
  "v_exp_f32 v87, v87\n\t" \
  "v_cvt_pk_bf16_f32 v80, v80, v81\n\t" \
  "v_cvt_pk_bf16_f32 v81, v82, v83\n\t" \
  "s_waitcnt lgkmcnt(2)\n\t" \
  "v_mfma_f32_32x32x16_bf16 %0, v[144:147], v[64:67], %0\n\t" \
  "v_exp_f32 v88, v88\n\t" \
  "v_exp_f32 v89, v89\n\t" \
  "v_exp_f32 v90, v90\n\t" \
  "v_exp_f32 v91, v91\n\t" \
  "v_cvt_pk_bf16_f32 v82, v84, v85\n\t" \
  "v_cvt_pk_bf16_f32 v83, v86, v87\n\t" \
  "v_mfma_f32_32x32x16_bf16 %0, v[148:151], v[68:71], %0\n\t" \
  "v_exp_f32 v92, v92\n\t" \
  "v_exp_f32 v93, v93\n\t" \
  "v_exp_f32 v94, v94\n\t" \
  "v_exp_f32 v95, v95\n\t" \
  "v_cvt_pk_bf16_f32 v84, v88, v89\n\t" \
  "v_cvt_pk_bf16_f32 v85, v90, v91\n\t" \
  "v_cvt_pk_bf16_f32 v86, v92, v93\n\t" \
  "v_cvt_pk_bf16_f32 v87, v94, v95\n\t" \
  "v_mfma_f32_32x32x16_bf16 v[112:127], v[128:131], %10, 0\n\t" \
  "v_exp_f32 v96, v96\n\t" \
  "v_exp_f32 v97, v97\n\t" \
  "v_exp_f32 v98, v98\n\t" \
  "v_exp_f32 v99, v99\n\t" \
  "v_mfma_f32_32x32x16_bf16 v[112:127], v[136:139], %14, v[112:127]\n\t" \
  "ds_read_b128 v[152:155], %6 offset:8512\n\t" \
  "ds_read_b128 v[156:159], %6 offset:9120\n\t" \
  "v_exp_f32 v100, v100\n\t" \
  "v_exp_f32 v101, v101\n\t" \
  "v_exp_f32 v102, v102\n\t" \
  "v_exp_f32 v103, v103\n\t" \
  "v_cvt_pk_bf16_f32 v96, v96, v97\n\t" \
  "v_cvt_pk_bf16_f32 v97, v98, v99\n\t" \
  "v_mfma_f32_32x32x16_bf16 %1, v[144:147], v[80:83], %1\n\t" \
  "v_exp_f32 v104, v104\n\t" \
  "v_exp_f32 v105, v105\n\t" \
  "v_exp_f32 v106, v106\n\t" \
  "v_exp_f32 v107, v107\n\t" \
  "v_cvt_pk_bf16_f32 v98, v100, v101\n\t" \
  "v_cvt_pk_bf16_f32 v99, v102, v103\n\t" \
  "v_mfma_f32_32x32x16_bf16 %1, v[148:151], v[84:87], %1\n\t" \
  "v_exp_f32 v108, v108\n\t" \
  "v_exp_f32 v109, v109\n\t" \
  "v_exp_f32 v110, v110\n\t" \
  "v_exp_f32 v111, v111\n\t" \
  "v_cvt_pk_bf16_f32 v100, v104, v105\n\t" \
  "v_cvt_pk_bf16_f32 v101, v106, v107\n\t" \
  "v_cvt_pk_bf16_f32 v102, v108, v109\n\t" \
  "v_cvt_pk_bf16_f32 v103, v110, v111\n\t" \
  "s_waitcnt lgkmcnt(2)\n\t" \
  "v_mfma_f32_32x32x16_bf16 v[64:79], v[132:135], %7, 0\n\t" \
  "v_exp_f32 v112, v112\n\t" \
  "v_exp_f32 v113, v113\n\t" \
  "v_exp_f32 v114, v114\n\t" \
  "v_exp_f32 v115, v115\n\t" \
  "v_mfma_f32_32x32x16_bf16 v[64:79], v[140:143], %11, v[64:79]\n\t" \
  "v_exp_f32 v116, v116\n\t" \
  "v_exp_f32 v117, v117\n\t" \
  "v_exp_f32 v118, v118\n\t" \
  "v_exp_f32 v119, v119\n\t" \
  "v_cvt_pk_bf16_f32 v112, v112, v113\n\t" \
  "v_cvt_pk_bf16_f32 v113, v114, v115\n\t" \
  "v_mfma_f32_32x32x16_bf16 %2, v[144:147], v[96:99], %2\n\t" \
  "v_exp_f32 v120, v120\n\t" \
  "v_exp_f32 v121, v121\n\t" \
  "v_exp_f32 v122, v122\n\t" \
  "v_exp_f32 v123, v123\n\t" \
  "v_cvt_pk_bf16_f32 v114, v116, v117\n\t" \
  "v_cvt_pk_bf16_f32 v115, v118, v119\n\t" \
  "v_mfma_f32_32x32x16_bf16 %2, v[148:151], v[100:103], %2\n\t" \
  "v_exp_f32 v124, v124\n\t" \
  "v_exp_f32 v125, v125\n\t" \
  "v_exp_f32 v126, v126\n\t" \
  "v_exp_f32 v127, v127\n\t" \
  "v_cvt_pk_bf16_f32 v116, v120, v121\n\t" \
  "v_cvt_pk_bf16_f32 v117, v122, v123\n\t" \
  "v_cvt_pk_bf16_f32 v118, v124, v125\n\t" \
  "v_cvt_pk_bf16_f32 v119, v126, v127\n\t" \
  "v_mfma_f32_32x32x16_bf16 v[80:95], v[132:135], %8, 0\n\t" \
  "ds_read_b128 v[128:131], %4 offset:18944\n\t" \
  "ds_read_b32 v136, %5 offset:18944\n\t" \
  "v_exp_f32 v64, v64\n\t" \
  "v_exp_f32 v65, v65\n\t" \
  "v_exp_f32 v66, v66\n\t" \
  "v_exp_f32 v67, v67\n\t" \
  "v_mfma_f32_32x32x16_bf16 v[80:95], v[140:143], %12, v[80:95]\n\t" \
  "v_exp_f32 v68, v68\n\t" \
  "v_exp_f32 v69, v69\n\t" \
  "v_exp_f32 v70, v70\n\t" \
  "v_exp_f32 v71, v71\n\t" \
  "v_cvt_pk_bf16_f32 v64, v64, v65\n\t" \
  "v_cvt_pk_bf16_f32 v65, v66, v67\n\t" \
  "v_mfma_f32_32x32x16_bf16 %3, v[144:147], v[112:115], %3\n\t" \
  "v_exp_f32 v72, v72\n\t" \
  "v_exp_f32 v73, v73\n\t" \
  "v_exp_f32 v74, v74\n\t" \
  "v_exp_f32 v75, v75\n\t" \
  "v_cvt_pk_bf16_f32 v66, v68, v69\n\t" \
  "v_cvt_pk_bf16_f32 v67, v70, v71\n\t" \
  "v_mfma_f32_32x32x16_bf16 %3, v[148:151], v[116:119], %3\n\t" \
  "v_exp_f32 v76, v76\n\t" \
  "v_exp_f32 v77, v77\n\t" \
  "v_exp_f32 v78, v78\n\t" \
  "v_exp_f32 v79, v79\n\t" \
  "v_cvt_pk_bf16_f32 v68, v72, v73\n\t" \
  "v_cvt_pk_bf16_f32 v69, v74, v75\n\t" \
  "v_cvt_pk_bf16_f32 v70, v76, v77\n\t" \
  "v_cvt_pk_bf16_f32 v71, v78, v79\n\t" \
  "v_mfma_f32_32x32x16_bf16 v[96:111], v[132:135], %9, 0\n\t" \
  "v_exp_f32 v80, v80\n\t" \
  "v_exp_f32 v81, v81\n\t" \
  "v_exp_f32 v82, v82\n\t" \
  "v_exp_f32 v83, v83\n\t" \
  "v_mfma_f32_32x32x16_bf16 v[96:111], v[140:143], %13, v[96:111]\n\t" \
  "v_exp_f32 v84, v84\n\t" \
  "v_exp_f32 v85, v85\n\t" \
  "v_exp_f32 v86, v86\n\t" \
  "v_exp_f32 v87, v87\n\t" \
  "v_cvt_pk_bf16_f32 v80, v80, v81\n\t" \
  "v_cvt_pk_bf16_f32 v81, v82, v83\n\t" \
  "s_waitcnt lgkmcnt(2)\n\t" \
  "v_mfma_f32_32x32x16_bf16 %0, v[152:155], v[64:67], %0\n\t" \
  "v_exp_f32 v88, v88\n\t" \
  "v_exp_f32 v89, v89\n\t" \
  "v_exp_f32 v90, v90\n\t" \
  "v_exp_f32 v91, v91\n\t" \
  "v_cvt_pk_bf16_f32 v82, v84, v85\n\t" \
  "v_cvt_pk_bf16_f32 v83, v86, v87\n\t" \
  "v_mfma_f32_32x32x16_bf16 %0, v[156:159], v[68:71], %0\n\t" \
  "v_exp_f32 v92, v92\n\t" \
  "v_exp_f32 v93, v93\n\t" \
  "v_exp_f32 v94, v94\n\t" \
  "v_exp_f32 v95, v95\n\t" \
  "v_cvt_pk_bf16_f32 v84, v88, v89\n\t" \
  "v_cvt_pk_bf16_f32 v85, v90, v91\n\t" \
  "v_cvt_pk_bf16_f32 v86, v92, v93\n\t" \
  "v_cvt_pk_bf16_f32 v87, v94, v95\n\t" \
  "v_mfma_f32_32x32x16_bf16 v[112:127], v[132:135], %10, 0\n\t" \
  "v_exp_f32 v96, v96\n\t" \
  "v_exp_f32 v97, v97\n\t" \
  "v_exp_f32 v98, v98\n\t" \
  "v_exp_f32 v99, v99\n\t" \
  "v_mfma_f32_32x32x16_bf16 v[112:127], v[140:143], %14, v[112:127]\n\t" \
  "ds_read_b128 v[144:147], %6 offset:18944\n\t" \
  "ds_read_b128 v[148:151], %6 offset:19552\n\t" \
  "v_exp_f32 v100, v100\n\t" \
  "v_exp_f32 v101, v101\n\t" \
  "v_exp_f32 v102, v102\n\t" \
  "v_exp_f32 v103, v103\n\t" \
  "v_cvt_pk_bf16_f32 v96, v96, v97\n\t" \
  "v_cvt_pk_bf16_f32 v97, v98, v99\n\t" \
  "v_mfma_f32_32x32x16_bf16 %1, v[152:155], v[80:83], %1\n\t" \
  "v_exp_f32 v104, v104\n\t" \
  "v_exp_f32 v105, v105\n\t" \
  "v_exp_f32 v106, v106\n\t" \
  "v_exp_f32 v107, v107\n\t" \
  "v_cvt_pk_bf16_f32 v98, v100, v101\n\t" \
  "v_cvt_pk_bf16_f32 v99, v102, v103\n\t" \
  "v_mfma_f32_32x32x16_bf16 %1, v[156:159], v[84:87], %1\n\t" \
  "v_exp_f32 v108, v108\n\t" \
  "v_exp_f32 v109, v109\n\t" \
  "v_exp_f32 v110, v110\n\t" \
  "v_exp_f32 v111, v111\n\t" \
  "v_cvt_pk_bf16_f32 v100, v104, v105\n\t" \
  "v_cvt_pk_bf16_f32 v101, v106, v107\n\t" \
  "v_cvt_pk_bf16_f32 v102, v108, v109\n\t" \
  "v_cvt_pk_bf16_f32 v103, v110, v111\n\t" \
  "s_waitcnt lgkmcnt(2)\n\t" \
  "v_mfma_f32_32x32x16_bf16 v[64:79], v[128:131], %7, 0\n\t" \
  "v_exp_f32 v112, v112\n\t" \
  "v_exp_f32 v113, v113\n\t" \
  "v_exp_f32 v114, v114\n\t" \
  "v_exp_f32 v115, v115\n\t" \
  "v_mfma_f32_32x32x16_bf16 v[64:79], v[136:139], %11, v[64:79]\n\t" \
  "v_exp_f32 v116, v116\n\t" \
  "v_exp_f32 v117, v117\n\t" \
  "v_exp_f32 v118, v118\n\t" \
  "v_exp_f32 v119, v119\n\t" \
  "v_cvt_pk_bf16_f32 v112, v112, v113\n\t" \
  "v_cvt_pk_bf16_f32 v113, v114, v115\n\t" \
  "v_mfma_f32_32x32x16_bf16 %2, v[152:155], v[96:99], %2\n\t" \
  "v_exp_f32 v120, v120\n\t" \
  "v_exp_f32 v121, v121\n\t" \
  "v_exp_f32 v122, v122\n\t" \
  "v_exp_f32 v123, v123\n\t" \
  "v_cvt_pk_bf16_f32 v114, v116, v117\n\t" \
  "v_cvt_pk_bf16_f32 v115, v118, v119\n\t" \
  "v_mfma_f32_32x32x16_bf16 %2, v[156:159], v[100:103], %2\n\t" \
  "v_exp_f32 v124, v124\n\t" \
  "v_exp_f32 v125, v125\n\t" \
  "v_exp_f32 v126, v126\n\t" \
  "v_exp_f32 v127, v127\n\t" \
  "v_cvt_pk_bf16_f32 v116, v120, v121\n\t" \
  "v_cvt_pk_bf16_f32 v117, v122, v123\n\t" \
  "v_cvt_pk_bf16_f32 v118, v124, v125\n\t" \
  "v_cvt_pk_bf16_f32 v119, v126, v127\n\t" \
  "v_add_u32 %4, 18944, %4\n\t" \
  "v_add_u32 %5, 18944, %5\n\t" \
  "v_add_u32 %6, 18944, %6\n\t" \
  "s_add_u32 s40, s40, 1\n\t" \
  "s_cmp_lg_u32 s40, 8\n\t" \
  "s_cbranch_scc1 ATT_LOOPB_%=\n\t" \
  "s_nop 1\n\t" \
  "v_mfma_f32_32x32x16_bf16 %3, v[152:155], v[112:115], %3\n\t" \
  "v_mfma_f32_32x32x16_bf16 %3, v[156:159], v[116:119], %3\n\t" \
  "s_waitcnt lgkmcnt(0)\n\t" \
  "s_nop 15\n\t" \
  "s_nop 7\n\t"
#define ATT_ASM_CLOB "v64", "v65", "v66", "v67", "v68", "v69", "v70", "v71", "v72", "v73", "v74", "v75", "v76", "v77", "v78", "v79", "v80", "v81", "v82", "v83", "v84", "v85", "v86", "v87", "v88", "v89", "v90", "v91", "v92", "v93", "v94", "v95", "v96", "v97", "v98", "v99", "v100", "v101", "v102", "v103", "v104", "v105", "v106", "v107", "v108", "v109", "v110", "v111", "v112", "v113", "v114", "v115", "v116", "v117", "v118", "v119", "v120", "v121", "v122", "v123", "v124", "v125", "v126", "v127", "v128", "v129", "v130", "v131", "v132", "v133", "v134", "v135", "v136", "v137", "v138", "v139", "v140", "v141", "v142", "v143", "v144", "v145", "v146", "v147", "v148", "v149", "v150", "v151", "v152", "v153", "v154", "v155", "v156", "v157", "v158", "v159", "v160", "s40", "s41", "s42", "s43", "s44", "scc", "memory"

#define MLP_ASM_TEXT \
  "v_mbcnt_lo_u32_b32 v252, -1, 0\n\t" \
  "v_mbcnt_hi_u32_b32 v252, -1, v252\n\t" \
  "s_mov_b32 s60, %5\n\t" \
  "s_mov_b64 s[40:41], %1\n\t" \
  "s_and_b32 s41, s41, 0xffff\n\t" \
  "s_mov_b32 s42, 5308416\n\t" \
  "s_mov_b32 s43, 0x20000\n\t" \
  "s_mov_b64 s[44:45], %2\n\t" \
  "s_and_b32 s45, s45, 0xffff\n\t" \
  "s_mov_b32 s46, 5308416\n\t" \
  "s_mov_b32 s47, 0x20000\n\t" \
  "s_mov_b64 s[48:49], %3\n\t" \
  "s_and_b32 s49, s49, 0xffff\n\t" \
  "s_mov_b32 s50, 2147483647\n\t" \
  "s_mov_b32 s51, 0x20000\n\t" \
  "v_lshrrev_b32 v232, 5, v252\n\t" \
  "v_bfe_u32 v233, v252, 2, 2\n\t" \
  "v_bfe_u32 v234, v252, 4, 1\n\t" \
  "v_and_b32 v235, 3, v252\n\t" \
  "v_bfe_u32 v236, v252, 3, 1\n\t" \
  "v_lshlrev_b32 v237, 5, v234\n\t" \
  "v_lshl_add_u32 v237, v235, 3, v237\n\t" \
  "v_lshl_add_u32 v238, v236, 6, v237\n\t" \
  "v_lshl_add_u32 v239, v232, 3, v233\n\t" \
  "v_lshl_add_u32 v245, v239, 7, v238\n\t" \
  "v_lshl_add_u32 v239, v232, 2, v233\n\t" \
  "v_mul_u32_u24 v246, 1088, v239\n\t" \
  "v_add_u32 v246, v246, v237\n\t" \
  "v_add_u32 v246, 73728, v246\n\t" \
  "v_lshl_add_u32 v247, v239, 7, v238\n\t" \
  "v_add_u32 v247, 143360, v247\n\t" \
  "v_xor_b32 v248, 64, v247\n\t" \
  "v_lshrrev_b32 v232, 3, v252\n\t" \
  "v_and_b32 v233, 7, v252\n\t" \
  "v_lshlrev_b32 v235, 2, v234\n\t" \
  "v_xor_b32 v233, v233, v235\n\t" \
  "v_lshlrev_b32 v233, 4, v233\n\t" \
  "v_mul_u32_u24 v249, 9216, v232\n\t" \
  "v_add_u32 v249, v249, v233\n\t" \
  "v_mul_u32_u24 v251, 1152, v232\n\t" \
  "v_add_u32 v251, v251, v233\n\t" \
  "v_add_u32 v251, 1024, v251\n\t" \
  "v_lshlrev_b32 v250, 4, v252\n\t" \
  "s_mul_i32 s52, s60, 663552\n\t" \
  "s_lshl_b32 s57, %4, 1\n\t" \
  "s_add_u32 s52, s52, s57\n\t" \
  "s_lshl_b32 s57, s60, 3\n\t" \
  "s_add_u32 s57, s57, %4\n\t" \
  "s_mul_i32 s53, s57, 1152\n\t" \
  "s_mul_i32 s54, s60, 9216\n\t" \
  "s_mul_i32 s55, s60, 8704\n\t" \
  "s_add_u32 s55, s55, 73728\n\t" \
  "s_lshl_b32 s56, s60, 10\n\t" \
  "s_add_u32 s56, s56, 143360\n\t" \
  "v_mov_b32 v244, 0xc0135761\n\t" \
  "s_mov_b32 s58, 0xbdd2d3e8\n\t" \
  "v_accvgpr_write_b32 a0, 0\n\t" \
  "v_accvgpr_write_b32 a1, 0\n\t" \
  "v_accvgpr_write_b32 a2, 0\n\t" \
  "v_accvgpr_write_b32 a3, 0\n\t" \
  "v_accvgpr_write_b32 a4, 0\n\t" \
  "v_accvgpr_write_b32 a5, 0\n\t" \
  "v_accvgpr_write_b32 a6, 0\n\t" \
  "v_accvgpr_write_b32 a7, 0\n\t" \
  "v_accvgpr_write_b32 a8, 0\n\t" \
  "v_accvgpr_write_b32 a9, 0\n\t" \
  "v_accvgpr_write_b32 a10, 0\n\t" \
  "v_accvgpr_write_b32 a11, 0\n\t" \
  "v_accvgpr_write_b32 a12, 0\n\t" \
  "v_accvgpr_write_b32 a13, 0\n\t" \
  "v_accvgpr_write_b32 a14, 0\n\t" \
  "v_accvgpr_write_b32 a15, 0\n\t" \
  "v_accvgpr_write_b32 a16, 0\n\t" \
  "v_accvgpr_write_b32 a17, 0\n\t" \
  "v_accvgpr_write_b32 a18, 0\n\t" \
  "v_accvgpr_write_b32 a19, 0\n\t" \
  "v_accvgpr_write_b32 a20, 0\n\t" \
  "v_accvgpr_write_b32 a21, 0\n\t" \
  "v_accvgpr_write_b32 a22, 0\n\t" \
  "v_accvgpr_write_b32 a23, 0\n\t" \
  "v_accvgpr_write_b32 a24, 0\n\t" \
  "v_accvgpr_write_b32 a25, 0\n\t" \
  "v_accvgpr_write_b32 a26, 0\n\t" \
  "v_accvgpr_write_b32 a27, 0\n\t" \
  "v_accvgpr_write_b32 a28, 0\n\t" \
  "v_accvgpr_write_b32 a29, 0\n\t" \
  "v_accvgpr_write_b32 a30, 0\n\t" \
  "v_accvgpr_write_b32 a31, 0\n\t" \
  "v_accvgpr_write_b32 a32, 0\n\t" \
  "v_accvgpr_write_b32 a33, 0\n\t" \
  "v_accvgpr_write_b32 a34, 0\n\t" \
  "v_accvgpr_write_b32 a35, 0\n\t" \
  "v_accvgpr_write_b32 a36, 0\n\t" \
  "v_accvgpr_write_b32 a37, 0\n\t" \
  "v_accvgpr_write_b32 a38, 0\n\t" \
  "v_accvgpr_write_b32 a39, 0\n\t" \
  "v_accvgpr_write_b32 a40, 0\n\t" \
  "v_accvgpr_write_b32 a41, 0\n\t" \
  "v_accvgpr_write_b32 a42, 0\n\t" \
  "v_accvgpr_write_b32 a43, 0\n\t" \
  "v_accvgpr_write_b32 a44, 0\n\t" \
  "v_accvgpr_write_b32 a45, 0\n\t" \
  "v_accvgpr_write_b32 a46, 0\n\t" \
  "v_accvgpr_write_b32 a47, 0\n\t" \
  "v_accvgpr_write_b32 a48, 0\n\t" \
  "v_accvgpr_write_b32 a49, 0\n\t" \
  "v_accvgpr_write_b32 a50, 0\n\t" \
  "v_accvgpr_write_b32 a51, 0\n\t" \
  "v_accvgpr_write_b32 a52, 0\n\t" \
  "v_accvgpr_write_b32 a53, 0\n\t" \
  "v_accvgpr_write_b32 a54, 0\n\t" \
  "v_accvgpr_write_b32 a55, 0\n\t" \
  "v_accvgpr_write_b32 a56, 0\n\t" \
  "v_accvgpr_write_b32 a57, 0\n\t" \
  "v_accvgpr_write_b32 a58, 0\n\t" \
  "v_accvgpr_write_b32 a59, 0\n\t" \
  "v_accvgpr_write_b32 a60, 0\n\t" \
  "v_accvgpr_write_b32 a61, 0\n\t" \
  "v_accvgpr_write_b32 a62, 0\n\t" \
  "v_accvgpr_write_b32 a63, 0\n\t" \
  "v_accvgpr_write_b32 a64, 0\n\t" \
  "v_accvgpr_write_b32 a65, 0\n\t" \
  "v_accvgpr_write_b32 a66, 0\n\t" \
  "v_accvgpr_write_b32 a67, 0\n\t" \
  "v_accvgpr_write_b32 a68, 0\n\t" \
  "v_accvgpr_write_b32 a69, 0\n\t" \
  "v_accvgpr_write_b32 a70, 0\n\t" \
  "v_accvgpr_write_b32 a71, 0\n\t" \
  "v_accvgpr_write_b32 a72, 0\n\t" \
  "v_accvgpr_write_b32 a73, 0\n\t" \
  "v_accvgpr_write_b32 a74, 0\n\t" \
  "v_accvgpr_write_b32 a75, 0\n\t" \
  "v_accvgpr_write_b32 a76, 0\n\t" \
  "v_accvgpr_write_b32 a77, 0\n\t" \
  "v_accvgpr_write_b32 a78, 0\n\t" \
  "v_accvgpr_write_b32 a79, 0\n\t" \
  "v_accvgpr_write_b32 a80, 0\n\t" \
  "v_accvgpr_write_b32 a81, 0\n\t" \
  "v_accvgpr_write_b32 a82, 0\n\t" \
  "v_accvgpr_write_b32 a83, 0\n\t" \
  "v_accvgpr_write_b32 a84, 0\n\t" \
  "v_accvgpr_write_b32 a85, 0\n\t" \
  "v_accvgpr_write_b32 a86, 0\n\t" \
  "v_accvgpr_write_b32 a87, 0\n\t" \
  "v_accvgpr_write_b32 a88, 0\n\t" \
  "v_accvgpr_write_b32 a89, 0\n\t" \
  "v_accvgpr_write_b32 a90, 0\n\t" \
  "v_accvgpr_write_b32 a91, 0\n\t" \
  "v_accvgpr_write_b32 a92, 0\n\t" \
  "v_accvgpr_write_b32 a93, 0\n\t" \
  "v_accvgpr_write_b32 a94, 0\n\t" \
  "v_accvgpr_write_b32 a95, 0\n\t" \
  "v_accvgpr_write_b32 a96, 0\n\t" \
  "v_accvgpr_write_b32 a97, 0\n\t" \
  "v_accvgpr_write_b32 a98, 0\n\t" \
  "v_accvgpr_write_b32 a99, 0\n\t" \
  "v_accvgpr_write_b32 a100, 0\n\t" \
  "v_accvgpr_write_b32 a101, 0\n\t" \
  "v_accvgpr_write_b32 a102, 0\n\t" \
  "v_accvgpr_write_b32 a103, 0\n\t" \
  "v_accvgpr_write_b32 a104, 0\n\t" \
  "v_accvgpr_write_b32 a105, 0\n\t" \
  "v_accvgpr_write_b32 a106, 0\n\t" \
  "v_accvgpr_write_b32 a107, 0\n\t" \
  "v_accvgpr_write_b32 a108, 0\n\t" \
  "v_accvgpr_write_b32 a109, 0\n\t" \
  "v_accvgpr_write_b32 a110, 0\n\t" \
  "v_accvgpr_write_b32 a111, 0\n\t" \
  "v_accvgpr_write_b32 a112, 0\n\t" \
  "v_accvgpr_write_b32 a113, 0\n\t" \
  "v_accvgpr_write_b32 a114, 0\n\t" \
  "v_accvgpr_write_b32 a115, 0\n\t" \
  "v_accvgpr_write_b32 a116, 0\n\t" \
  "v_accvgpr_write_b32 a117, 0\n\t" \
  "v_accvgpr_write_b32 a118, 0\n\t" \
  "v_accvgpr_write_b32 a119, 0\n\t" \
  "v_accvgpr_write_b32 a120, 0\n\t" \
  "v_accvgpr_write_b32 a121, 0\n\t" \
  "v_accvgpr_write_b32 a122, 0\n\t" \
  "v_accvgpr_write_b32 a123, 0\n\t" \
  "v_accvgpr_write_b32 a124, 0\n\t" \
  "v_accvgpr_write_b32 a125, 0\n\t" \
  "v_accvgpr_write_b32 a126, 0\n\t" \
  "v_accvgpr_write_b32 a127, 0\n\t" \
  "v_accvgpr_write_b32 a128, 0\n\t" \
  "v_accvgpr_write_b32 a129, 0\n\t" \
  "v_accvgpr_write_b32 a130, 0\n\t" \
  "v_accvgpr_write_b32 a131, 0\n\t" \
  "v_accvgpr_write_b32 a132, 0\n\t" \
  "v_accvgpr_write_b32 a133, 0\n\t" \
  "v_accvgpr_write_b32 a134, 0\n\t" \
  "v_accvgpr_write_b32 a135, 0\n\t" \
  "v_accvgpr_write_b32 a136, 0\n\t" \
  "v_accvgpr_write_b32 a137, 0\n\t" \
  "v_accvgpr_write_b32 a138, 0\n\t" \
  "v_accvgpr_write_b32 a139, 0\n\t" \
  "v_accvgpr_write_b32 a140, 0\n\t" \
  "v_accvgpr_write_b32 a141, 0\n\t" \
  "v_accvgpr_write_b32 a142, 0\n\t" \
  "v_accvgpr_write_b32 a143, 0\n\t" \
  "v_accvgpr_write_b32 a144, 0\n\t" \
  "v_accvgpr_write_b32 a145, 0\n\t" \
  "v_accvgpr_write_b32 a146, 0\n\t" \
  "v_accvgpr_write_b32 a147, 0\n\t" \
  "v_accvgpr_write_b32 a148, 0\n\t" \
  "v_accvgpr_write_b32 a149, 0\n\t" \
  "v_accvgpr_write_b32 a150, 0\n\t" \
  "v_accvgpr_write_b32 a151, 0\n\t" \
  "v_accvgpr_write_b32 a152, 0\n\t" \
  "v_accvgpr_write_b32 a153, 0\n\t" \
  "v_accvgpr_write_b32 a154, 0\n\t" \
  "v_accvgpr_write_b32 a155, 0\n\t" \
  "v_accvgpr_write_b32 a156, 0\n\t" \
  "v_accvgpr_write_b32 a157, 0\n\t" \
  "v_accvgpr_write_b32 a158, 0\n\t" \
  "v_accvgpr_write_b32 a159, 0\n\t" \
  "v_accvgpr_write_b32 a160, 0\n\t" \
  "v_accvgpr_write_b32 a161, 0\n\t" \
  "v_accvgpr_write_b32 a162, 0\n\t" \
  "v_accvgpr_write_b32 a163, 0\n\t" \
  "v_accvgpr_write_b32 a164, 0\n\t" \
  "v_accvgpr_write_b32 a165, 0\n\t" \
  "v_accvgpr_write_b32 a166, 0\n\t" \
  "v_accvgpr_write_b32 a167, 0\n\t" \
  "v_accvgpr_write_b32 a168, 0\n\t" \
  "v_accvgpr_write_b32 a169, 0\n\t" \
  "v_accvgpr_write_b32 a170, 0\n\t" \
  "v_accvgpr_write_b32 a171, 0\n\t" \
  "v_accvgpr_write_b32 a172, 0\n\t" \
  "v_accvgpr_write_b32 a173, 0\n\t" \
  "v_accvgpr_write_b32 a174, 0\n\t" \
  "v_accvgpr_write_b32 a175, 0\n\t" \
  "v_accvgpr_write_b32 a176, 0\n\t" \
  "v_accvgpr_write_b32 a177, 0\n\t" \
  "v_accvgpr_write_b32 a178, 0\n\t" \
  "v_accvgpr_write_b32 a179, 0\n\t" \
  "v_accvgpr_write_b32 a180, 0\n\t" \
  "v_accvgpr_write_b32 a181, 0\n\t" \
  "v_accvgpr_write_b32 a182, 0\n\t" \
  "v_accvgpr_write_b32 a183, 0\n\t" \
  "v_accvgpr_write_b32 a184, 0\n\t" \
  "v_accvgpr_write_b32 a185, 0\n\t" \
  "v_accvgpr_write_b32 a186, 0\n\t" \
  "v_accvgpr_write_b32 a187, 0\n\t" \
  "v_accvgpr_write_b32 a188, 0\n\t" \
  "v_accvgpr_write_b32 a189, 0\n\t" \
  "v_accvgpr_write_b32 a190, 0\n\t" \
  "v_accvgpr_write_b32 a191, 0\n\t" \
  "v_accvgpr_write_b32 a192, 0\n\t" \
  "v_accvgpr_write_b32 a193, 0\n\t" \
  "v_accvgpr_write_b32 a194, 0\n\t" \
  "v_accvgpr_write_b32 a195, 0\n\t" \
  "v_accvgpr_write_b32 a196, 0\n\t" \
  "v_accvgpr_write_b32 a197, 0\n\t" \
  "v_accvgpr_write_b32 a198, 0\n\t" \
  "v_accvgpr_write_b32 a199, 0\n\t" \
  "v_accvgpr_write_b32 a200, 0\n\t" \
  "v_accvgpr_write_b32 a201, 0\n\t" \
  "v_accvgpr_write_b32 a202, 0\n\t" \
  "v_accvgpr_write_b32 a203, 0\n\t" \
  "v_accvgpr_write_b32 a204, 0\n\t" \
  "v_accvgpr_write_b32 a205, 0\n\t" \
  "v_accvgpr_write_b32 a206, 0\n\t" \
  "v_accvgpr_write_b32 a207, 0\n\t" \
  "v_accvgpr_write_b32 a208, 0\n\t" \
  "v_accvgpr_write_b32 a209, 0\n\t" \
  "v_accvgpr_write_b32 a210, 0\n\t" \
  "v_accvgpr_write_b32 a211, 0\n\t" \
  "v_accvgpr_write_b32 a212, 0\n\t" \
  "v_accvgpr_write_b32 a213, 0\n\t" \
  "v_accvgpr_write_b32 a214, 0\n\t" \
  "v_accvgpr_write_b32 a215, 0\n\t" \
  "v_accvgpr_write_b32 a216, 0\n\t" \
  "v_accvgpr_write_b32 a217, 0\n\t" \
  "v_accvgpr_write_b32 a218, 0\n\t" \
  "v_accvgpr_write_b32 a219, 0\n\t" \
  "v_accvgpr_write_b32 a220, 0\n\t" \
  "v_accvgpr_write_b32 a221, 0\n\t" \
  "v_accvgpr_write_b32 a222, 0\n\t" \
  "v_accvgpr_write_b32 a223, 0\n\t" \
  "v_accvgpr_write_b32 a224, 0\n\t" \
  "v_accvgpr_write_b32 a225, 0\n\t" \
  "v_accvgpr_write_b32 a226, 0\n\t" \
  "v_accvgpr_write_b32 a227, 0\n\t" \
  "v_accvgpr_write_b32 a228, 0\n\t" \
  "v_accvgpr_write_b32 a229, 0\n\t" \
  "v_accvgpr_write_b32 a230, 0\n\t" \
  "v_accvgpr_write_b32 a231, 0\n\t" \
  "v_accvgpr_write_b32 a232, 0\n\t" \
  "v_accvgpr_write_b32 a233, 0\n\t" \
  "v_accvgpr_write_b32 a234, 0\n\t" \
  "v_accvgpr_write_b32 a235, 0\n\t" \
  "v_accvgpr_write_b32 a236, 0\n\t" \
  "v_accvgpr_write_b32 a237, 0\n\t" \
  "v_accvgpr_write_b32 a238, 0\n\t" \
  "v_accvgpr_write_b32 a239, 0\n\t" \
  "v_accvgpr_write_b32 a240, 0\n\t" \
  "v_accvgpr_write_b32 a241, 0\n\t" \
  "v_accvgpr_write_b32 a242, 0\n\t" \
  "v_accvgpr_write_b32 a243, 0\n\t" \
  "v_accvgpr_write_b32 a244, 0\n\t" \
  "v_accvgpr_write_b32 a245, 0\n\t" \
  "v_accvgpr_write_b32 a246, 0\n\t" \
  "v_accvgpr_write_b32 a247, 0\n\t" \
  "v_accvgpr_write_b32 a248, 0\n\t" \
  "v_accvgpr_write_b32 a249, 0\n\t" \
  "v_accvgpr_write_b32 a250, 0\n\t" \
  "v_accvgpr_write_b32 a251, 0\n\t" \
  "v_accvgpr_write_b32 a252, 0\n\t" \
  "v_accvgpr_write_b32 a253, 0\n\t" \
  "v_accvgpr_write_b32 a254, 0\n\t" \
  "v_accvgpr_write_b32 a255, 0\n\t" \
  "v_mov_b32 v0, 0\n\t" \
  "v_mov_b32 v1, 0\n\t" \
  "v_mov_b32 v2, 0\n\t" \
  "v_mov_b32 v3, 0\n\t" \
  "v_mov_b32 v4, 0\n\t" \
  "v_mov_b32 v5, 0\n\t" \
  "v_mov_b32 v6, 0\n\t" \
  "v_mov_b32 v7, 0\n\t" \
  "v_mov_b32 v8, 0\n\t" \
  "v_mov_b32 v9, 0\n\t" \
  "v_mov_b32 v10, 0\n\t" \
  "v_mov_b32 v11, 0\n\t" \
  "v_mov_b32 v12, 0\n\t" \
  "v_mov_b32 v13, 0\n\t" \
  "v_mov_b32 v14, 0\n\t" \
  "v_mov_b32 v15, 0\n\t" \
  "v_mov_b32 v16, 0\n\t" \
  "v_mov_b32 v17, 0\n\t" \
  "v_mov_b32 v18, 0\n\t" \
  "v_mov_b32 v19, 0\n\t" \
  "v_mov_b32 v20, 0\n\t" \
  "v_mov_b32 v21, 0\n\t" \
  "v_mov_b32 v22, 0\n\t" \
  "v_mov_b32 v23, 0\n\t" \
  "v_mov_b32 v24, 0\n\t" \
  "v_mov_b32 v25, 0\n\t" \
  "v_mov_b32 v26, 0\n\t" \
  "v_mov_b32 v27, 0\n\t" \
  "v_mov_b32 v28, 0\n\t" \
  "v_mov_b32 v29, 0\n\t" \
  "v_mov_b32 v30, 0\n\t" \
  "v_mov_b32 v31, 0\n\t" \
  "s_cmp_lg_u32 s60, 0\n\t" \
  "s_cbranch_scc1 MLP_HSKIP_%=\n\t" \
  "v_mov_b32 v254, 0\n\t" \
  "s_mov_b32 s63, 0\n\t" \
  "MLP_HPOLL_%=:\n\t" \
  "global_load_dword v255, v254, %8 sc1\n\t" \
  "s_waitcnt vmcnt(0)\n\t" \
  "v_readfirstlane_b32 s57, v255\n\t" \
  "s_nop 3\n\t" \
  "s_cmp_ge_u32 s57, 8\n\t" \
  "s_cbranch_scc1 MLP_HSKIP_%=\n\t" \
  "s_sleep 2\n\t" \
  "s_add_u32 s63, s63, 1\n\t" \
  "s_cmp_lt_u32 s63, 0x10000\n\t" \
  "s_cbranch_scc1 MLP_HPOLL_%=\n\t" \
  "MLP_HSKIP_%=:\n\t" \
  "s_barrier\n\t" \
  "v_and_b32 v254, 31, v252\n\t" \
  "v_lshlrev_b32 v254, 7, v254\n\t" \
  "global_load_dwordx4 v[176:179], v254, %6 offset:0 sc1\n\t" \
  "global_load_dwordx4 v[180:183], v254, %6 offset:16 sc1\n\t" \
  "global_load_dwordx4 v[184:187], v254, %6 offset:32 sc1\n\t" \
  "global_load_dwordx4 v[188:191], v254, %6 offset:48 sc1\n\t" \
  "v_lshlrev_b32 v253, 4, v252\n\t" \
  "global_load_dwordx4 v[32:35], v253, %0 offset:0 sc1\n\t" \
  "global_load_dwordx4 v[36:39], v253, %0 offset:1024 sc1\n\t" \
  "global_load_dwordx4 v[40:43], v253, %0 offset:2048 sc1\n\t" \
  "global_load_dwordx4 v[44:47], v253, %0 offset:3072 sc1\n\t" \
  "v_add_u32 v253, 4096, v253\n\t" \
  "global_load_dwordx4 v[48:51], v253, %0 offset:0 sc1\n\t" \
  "global_load_dwordx4 v[52:55], v253, %0 offset:1024 sc1\n\t" \
  "global_load_dwordx4 v[56:59], v253, %0 offset:2048 sc1\n\t" \
  "global_load_dwordx4 v[60:63], v253, %0 offset:3072 sc1\n\t" \
  "v_add_u32 v253, 4096, v253\n\t" \
  "global_load_dwordx4 v[64:67], v253, %0 offset:0 sc1\n\t" \
  "global_load_dwordx4 v[68:71], v253, %0 offset:1024 sc1\n\t" \
  "global_load_dwordx4 v[72:75], v253, %0 offset:2048 sc1\n\t" \
  "global_load_dwordx4 v[76:79], v253, %0 offset:3072 sc1\n\t" \
  "v_add_u32 v253, 4096, v253\n\t" \
  "global_load_dwordx4 v[80:83], v253, %0 offset:0 sc1\n\t" \
  "global_load_dwordx4 v[84:87], v253, %0 offset:1024 sc1\n\t" \
  "global_load_dwordx4 v[88:91], v253, %0 offset:2048 sc1\n\t" \
  "global_load_dwordx4 v[92:95], v253, %0 offset:3072 sc1\n\t" \
  "v_add_u32 v253, 4096, v253\n\t" \
  "global_load_dwordx4 v[96:99], v253, %0 offset:0 sc1\n\t" \
  "global_load_dwordx4 v[100:103], v253, %0 offset:1024 sc1\n\t" \
  "global_load_dwordx4 v[104:107], v253, %0 offset:2048 sc1\n\t" \
  "global_load_dwordx4 v[108:111], v253, %0 offset:3072 sc1\n\t" \
  "v_add_u32 v253, 4096, v253\n\t" \
  "global_load_dwordx4 v[112:115], v253, %0 offset:0 sc1\n\t" \
  "global_load_dwordx4 v[116:119], v253, %0 offset:1024 sc1\n\t" \
  "global_load_dwordx4 v[120:123], v253, %0 offset:2048 sc1\n\t" \
  "global_load_dwordx4 v[124:127], v253, %0 offset:3072 sc1\n\t" \
  "s_waitcnt vmcnt(24)\n\t" \
  "v_add_f32 v176, v176, v184\n\t" \
  "v_add_f32 v177, v177, v185\n\t" \
  "v_add_f32 v178, v178, v186\n\t" \
  "v_add_f32 v179, v179, v187\n\t" \
  "v_add_f32 v180, v180, v188\n\t" \
  "v_add_f32 v181, v181, v189\n\t" \
  "v_add_f32 v182, v182, v190\n\t" \
  "v_add_f32 v183, v183, v191\n\t" \
  "v_add_f32 v176, v176, v180\n\t" \
  "v_add_f32 v177, v177, v181\n\t" \
  "v_add_f32 v178, v178, v182\n\t" \
  "v_add_f32 v179, v179, v183\n\t" \
  "v_add_f32 v176, v176, v178\n\t" \
  "v_add_f32 v177, v177, v179\n\t" \
  "v_mul_f32 v240, 0x3ae38e39, v176\n\t" \
  "v_mul_f32 v241, 0x3ae38e39, v177\n\t" \
  "v_fma_f32 v241, -v240, v240, v241\n\t" \
  "v_max_f32 v241, 0, v241\n\t" \
  "v_add_f32 v241, 0x3727c5ac, v241\n\t" \
  "v_rsq_f32 v241, v241\n\t" \
  "s_nop 1\n\t" \
  "v_mul_f32 v242, v240, v241\n\t" \
  "v_sub_f32 v242, 0, v242\n\t" \
  "s_waitcnt vmcnt(22)\n\t" \
  "v_lshlrev_b32 v232, 16, v32\n\t" \
  "v_lshlrev_b32 v234, 16, v33\n\t" \
  "v_lshlrev_b32 v236, 16, v34\n\t" \
  "v_lshlrev_b32 v238, 16, v35\n\t" \
  "v_and_b32 v233, 0xffff0000, v32\n\t" \
  "v_and_b32 v235, 0xffff0000, v33\n\t" \
  "v_and_b32 v237, 0xffff0000, v34\n\t" \
  "v_and_b32 v239, 0xffff0000, v35\n\t" \
  "v_fma_f32 v232, v232, v241, v242\n\t" \
  "v_fma_f32 v233, v233, v241, v242\n\t" \
  "v_fma_f32 v234, v234, v241, v242\n\t" \
  "v_fma_f32 v235, v235, v241, v242\n\t" \
  "v_fma_f32 v236, v236, v241, v242\n\t" \
  "v_fma_f32 v237, v237, v241, v242\n\t" \
  "v_fma_f32 v238, v238, v241, v242\n\t" \
  "v_fma_f32 v239, v239, v241, v242\n\t" \
  "v_cvt_pk_bf16_f32 v32, v232, v233\n\t" \
  "v_cvt_pk_bf16_f32 v33, v234, v235\n\t" \
  "v_cvt_pk_bf16_f32 v34, v236, v237\n\t" \
  "v_cvt_pk_bf16_f32 v35, v238, v239\n\t" \
  "v_add_u32 v253, 4096, v253\n\t" \
  "global_load_dwordx4 v[128:131], v253, %0 offset:0 sc1\n\t" \
  "v_lshlrev_b32 v232, 16, v36\n\t" \
  "v_lshlrev_b32 v234, 16, v37\n\t" \
  "v_lshlrev_b32 v236, 16, v38\n\t" \
  "v_lshlrev_b32 v238, 16, v39\n\t" \
  "v_and_b32 v233, 0xffff0000, v36\n\t" \
  "v_and_b32 v235, 0xffff0000, v37\n\t" \
  "v_and_b32 v237, 0xffff0000, v38\n\t" \
  "v_and_b32 v239, 0xffff0000, v39\n\t" \
  "v_fma_f32 v232, v232, v241, v242\n\t" \
  "v_fma_f32 v233, v233, v241, v242\n\t" \
  "v_fma_f32 v234, v234, v241, v242\n\t" \
  "v_fma_f32 v235, v235, v241, v242\n\t" \
  "v_fma_f32 v236, v236, v241, v242\n\t" \
  "v_fma_f32 v237, v237, v241, v242\n\t" \
  "v_fma_f32 v238, v238, v241, v242\n\t" \
  "v_fma_f32 v239, v239, v241, v242\n\t" \
  "v_cvt_pk_bf16_f32 v36, v232, v233\n\t" \
  "v_cvt_pk_bf16_f32 v37, v234, v235\n\t" \
  "v_cvt_pk_bf16_f32 v38, v236, v237\n\t" \
  "v_cvt_pk_bf16_f32 v39, v238, v239\n\t" \
  "global_load_dwordx4 v[132:135], v253, %0 offset:1024 sc1\n\t" \
  "s_waitcnt vmcnt(22)\n\t" \
  "v_lshlrev_b32 v232, 16, v40\n\t" \
  "v_lshlrev_b32 v234, 16, v41\n\t" \
  "v_lshlrev_b32 v236, 16, v42\n\t" \
  "v_lshlrev_b32 v238, 16, v43\n\t" \
  "v_and_b32 v233, 0xffff0000, v40\n\t" \
  "v_and_b32 v235, 0xffff0000, v41\n\t" \
  "v_and_b32 v237, 0xffff0000, v42\n\t" \
  "v_and_b32 v239, 0xffff0000, v43\n\t" \
  "v_fma_f32 v232, v232, v241, v242\n\t" \
  "v_fma_f32 v233, v233, v241, v242\n\t" \
  "v_fma_f32 v234, v234, v241, v242\n\t" \
  "v_fma_f32 v235, v235, v241, v242\n\t" \
  "v_fma_f32 v236, v236, v241, v242\n\t" \
  "v_fma_f32 v237, v237, v241, v242\n\t" \
  "v_fma_f32 v238, v238, v241, v242\n\t" \
  "v_fma_f32 v239, v239, v241, v242\n\t" \
  "v_cvt_pk_bf16_f32 v40, v232, v233\n\t" \
  "v_cvt_pk_bf16_f32 v41, v234, v235\n\t" \
  "v_cvt_pk_bf16_f32 v42, v236, v237\n\t" \
  "v_cvt_pk_bf16_f32 v43, v238, v239\n\t" \
  "global_load_dwordx4 v[136:139], v253, %0 offset:2048 sc1\n\t" \
  "v_lshlrev_b32 v232, 16, v44\n\t" \
  "v_lshlrev_b32 v234, 16, v45\n\t" \
  "v_lshlrev_b32 v236, 16, v46\n\t" \
  "v_lshlrev_b32 v238, 16, v47\n\t" \
  "v_and_b32 v233, 0xffff0000, v44\n\t" \
  "v_and_b32 v235, 0xffff0000, v45\n\t" \
  "v_and_b32 v237, 0xffff0000, v46\n\t" \
  "v_and_b32 v239, 0xffff0000, v47\n\t" \
  "v_fma_f32 v232, v232, v241, v242\n\t" \
  "v_fma_f32 v233, v233, v241, v242\n\t" \
  "v_fma_f32 v234, v234, v241, v242\n\t" \
  "v_fma_f32 v235, v235, v241, v242\n\t" \
  "v_fma_f32 v236, v236, v241, v242\n\t" \
  "v_fma_f32 v237, v237, v241, v242\n\t" \
  "v_fma_f32 v238, v238, v241, v242\n\t" \
  "v_fma_f32 v239, v239, v241, v242\n\t" \
  "v_cvt_pk_bf16_f32 v44, v232, v233\n\t" \
  "v_cvt_pk_bf16_f32 v45, v234, v235\n\t" \
  "v_cvt_pk_bf16_f32 v46, v236, v237\n\t" \
  "v_cvt_pk_bf16_f32 v47, v238, v239\n\t" \
  "global_load_dwordx4 v[140:143], v253, %0 offset:3072 sc1\n\t" \
  "s_waitcnt vmcnt(22)\n\t" \
  "v_lshlrev_b32 v232, 16, v48\n\t" \
  "v_lshlrev_b32 v234, 16, v49\n\t" \
  "v_lshlrev_b32 v236, 16, v50\n\t" \
  "v_lshlrev_b32 v238, 16, v51\n\t" \
  "v_and_b32 v233, 0xffff0000, v48\n\t" \
  "v_and_b32 v235, 0xffff0000, v49\n\t" \
  "v_and_b32 v237, 0xffff0000, v50\n\t" \
  "v_and_b32 v239, 0xffff0000, v51\n\t" \
  "v_fma_f32 v232, v232, v241, v242\n\t" \
  "v_fma_f32 v233, v233, v241, v242\n\t" \
  "v_fma_f32 v234, v234, v241, v242\n\t" \
  "v_fma_f32 v235, v235, v241, v242\n\t" \
  "v_fma_f32 v236, v236, v241, v242\n\t" \
  "v_fma_f32 v237, v237, v241, v242\n\t" \
  "v_fma_f32 v238, v238, v241, v242\n\t" \
  "v_fma_f32 v239, v239, v241, v242\n\t" \
  "v_cvt_pk_bf16_f32 v48, v232, v233\n\t" \
  "v_cvt_pk_bf16_f32 v49, v234, v235\n\t" \
  "v_cvt_pk_bf16_f32 v50, v236, v237\n\t" \
  "v_cvt_pk_bf16_f32 v51, v238, v239\n\t" \
  "v_add_u32 v253, 4096, v253\n\t" \
  "global_load_dwordx4 v[144:147], v253, %0 offset:0 sc1\n\t" \
  "v_lshlrev_b32 v232, 16, v52\n\t" \
  "v_lshlrev_b32 v234, 16, v53\n\t" \
  "v_lshlrev_b32 v236, 16, v54\n\t" \
  "v_lshlrev_b32 v238, 16, v55\n\t" \
  "v_and_b32 v233, 0xffff0000, v52\n\t" \
  "v_and_b32 v235, 0xffff0000, v53\n\t" \
  "v_and_b32 v237, 0xffff0000, v54\n\t" \
  "v_and_b32 v239, 0xffff0000, v55\n\t" \
  "v_fma_f32 v232, v232, v241, v242\n\t" \
  "v_fma_f32 v233, v233, v241, v242\n\t" \
  "v_fma_f32 v234, v234, v241, v242\n\t" \
  "v_fma_f32 v235, v235, v241, v242\n\t" \
  "v_fma_f32 v236, v236, v241, v242\n\t" \
  "v_fma_f32 v237, v237, v241, v242\n\t" \
  "v_fma_f32 v238, v238, v241, v242\n\t" \
  "v_fma_f32 v239, v239, v241, v242\n\t" \
  "v_cvt_pk_bf16_f32 v52, v232, v233\n\t" \
  "v_cvt_pk_bf16_f32 v53, v234, v235\n\t" \
  "v_cvt_pk_bf16_f32 v54, v236, v237\n\t" \
  "v_cvt_pk_bf16_f32 v55, v238, v239\n\t" \
  "global_load_dwordx4 v[148:151], v253, %0 offset:1024 sc1\n\t" \
  "s_waitcnt vmcnt(22)\n\t" \
  "v_lshlrev_b32 v232, 16, v56\n\t" \
  "v_lshlrev_b32 v234, 16, v57\n\t" \
  "v_lshlrev_b32 v236, 16, v58\n\t" \
  "v_lshlrev_b32 v238, 16, v59\n\t" \
  "v_and_b32 v233, 0xffff0000, v56\n\t" \
  "v_and_b32 v235, 0xffff0000, v57\n\t" \
  "v_and_b32 v237, 0xffff0000, v58\n\t" \
  "v_and_b32 v239, 0xffff0000, v59\n\t" \
  "v_fma_f32 v232, v232, v241, v242\n\t" \
  "v_fma_f32 v233, v233, v241, v242\n\t" \
  "v_fma_f32 v234, v234, v241, v242\n\t" \
  "v_fma_f32 v235, v235, v241, v242\n\t" \
  "v_fma_f32 v236, v236, v241, v242\n\t" \
  "v_fma_f32 v237, v237, v241, v242\n\t" \
  "v_fma_f32 v238, v238, v241, v242\n\t" \
  "v_fma_f32 v239, v239, v241, v242\n\t" \
  "v_cvt_pk_bf16_f32 v56, v232, v233\n\t" \
  "v_cvt_pk_bf16_f32 v57, v234, v235\n\t" \
  "v_cvt_pk_bf16_f32 v58, v236, v237\n\t" \
  "v_cvt_pk_bf16_f32 v59, v238, v239\n\t" \
  "global_load_dwordx4 v[152:155], v253, %0 offset:2048 sc1\n\t" \
  "v_lshlrev_b32 v232, 16, v60\n\t" \
  "v_lshlrev_b32 v234, 16, v61\n\t" \
  "v_lshlrev_b32 v236, 16, v62\n\t" \
  "v_lshlrev_b32 v238, 16, v63\n\t" \
  "v_and_b32 v233, 0xffff0000, v60\n\t" \
  "v_and_b32 v235, 0xffff0000, v61\n\t" \
  "v_and_b32 v237, 0xffff0000, v62\n\t" \
  "v_and_b32 v239, 0xffff0000, v63\n\t" \
  "v_fma_f32 v232, v232, v241, v242\n\t" \
  "v_fma_f32 v233, v233, v241, v242\n\t" \
  "v_fma_f32 v234, v234, v241, v242\n\t" \
  "v_fma_f32 v235, v235, v241, v242\n\t" \
  "v_fma_f32 v236, v236, v241, v242\n\t" \
  "v_fma_f32 v237, v237, v241, v242\n\t" \
  "v_fma_f32 v238, v238, v241, v242\n\t" \
  "v_fma_f32 v239, v239, v241, v242\n\t" \
  "v_cvt_pk_bf16_f32 v60, v232, v233\n\t" \
  "v_cvt_pk_bf16_f32 v61, v234, v235\n\t" \
  "v_cvt_pk_bf16_f32 v62, v236, v237\n\t" \
  "v_cvt_pk_bf16_f32 v63, v238, v239\n\t" \
  "global_load_dwordx4 v[156:159], v253, %0 offset:3072 sc1\n\t" \
  "s_waitcnt vmcnt(22)\n\t" \
  "v_lshlrev_b32 v232, 16, v64\n\t" \
  "v_lshlrev_b32 v234, 16, v65\n\t" \
  "v_lshlrev_b32 v236, 16, v66\n\t" \
  "v_lshlrev_b32 v238, 16, v67\n\t" \
  "v_and_b32 v233, 0xffff0000, v64\n\t" \
  "v_and_b32 v235, 0xffff0000, v65\n\t" \
  "v_and_b32 v237, 0xffff0000, v66\n\t" \
  "v_and_b32 v239, 0xffff0000, v67\n\t" \
  "v_fma_f32 v232, v232, v241, v242\n\t" \
  "v_fma_f32 v233, v233, v241, v242\n\t" \
  "v_fma_f32 v234, v234, v241, v242\n\t" \
  "v_fma_f32 v235, v235, v241, v242\n\t" \
  "v_fma_f32 v236, v236, v241, v242\n\t" \
  "v_fma_f32 v237, v237, v241, v242\n\t" \
  "v_fma_f32 v238, v238, v241, v242\n\t" \
  "v_fma_f32 v239, v239, v241, v242\n\t" \
  "v_cvt_pk_bf16_f32 v64, v232, v233\n\t" \
  "v_cvt_pk_bf16_f32 v65, v234, v235\n\t" \
  "v_cvt_pk_bf16_f32 v66, v236, v237\n\t" \
  "v_cvt_pk_bf16_f32 v67, v238, v239\n\t" \
  "v_add_u32 v253, 4096, v253\n\t" \
  "global_load_dwordx4 v[160:163], v253, %0 offset:0 sc1\n\t" \
  "v_lshlrev_b32 v232, 16, v68\n\t" \
  "v_lshlrev_b32 v234, 16, v69\n\t" \
  "v_lshlrev_b32 v236, 16, v70\n\t" \
  "v_lshlrev_b32 v238, 16, v71\n\t" \
  "v_and_b32 v233, 0xffff0000, v68\n\t" \
  "v_and_b32 v235, 0xffff0000, v69\n\t" \
  "v_and_b32 v237, 0xffff0000, v70\n\t" \
  "v_and_b32 v239, 0xffff0000, v71\n\t" \
  "v_fma_f32 v232, v232, v241, v242\n\t" \
  "v_fma_f32 v233, v233, v241, v242\n\t" \
  "v_fma_f32 v234, v234, v241, v242\n\t" \
  "v_fma_f32 v235, v235, v241, v242\n\t" \
  "v_fma_f32 v236, v236, v241, v242\n\t" \
  "v_fma_f32 v237, v237, v241, v242\n\t" \
  "v_fma_f32 v238, v238, v241, v242\n\t" \
  "v_fma_f32 v239, v239, v241, v242\n\t" \
  "v_cvt_pk_bf16_f32 v68, v232, v233\n\t" \
  "v_cvt_pk_bf16_f32 v69, v234, v235\n\t" \
  "v_cvt_pk_bf16_f32 v70, v236, v237\n\t" \
  "v_cvt_pk_bf16_f32 v71, v238, v239\n\t" \
  "global_load_dwordx4 v[164:167], v253, %0 offset:1024 sc1\n\t" \
  "s_waitcnt vmcnt(22)\n\t" \
  "v_lshlrev_b32 v232, 16, v72\n\t" \
  "v_lshlrev_b32 v234, 16, v73\n\t" \
  "v_lshlrev_b32 v236, 16, v74\n\t" \
  "v_lshlrev_b32 v238, 16, v75\n\t" \
  "v_and_b32 v233, 0xffff0000, v72\n\t" \
  "v_and_b32 v235, 0xffff0000, v73\n\t" \
  "v_and_b32 v237, 0xffff0000, v74\n\t" \
  "v_and_b32 v239, 0xffff0000, v75\n\t" \
  "v_fma_f32 v232, v232, v241, v242\n\t" \
  "v_fma_f32 v233, v233, v241, v242\n\t" \
  "v_fma_f32 v234, v234, v241, v242\n\t" \
  "v_fma_f32 v235, v235, v241, v242\n\t" \
  "v_fma_f32 v236, v236, v241, v242\n\t" \
  "v_fma_f32 v237, v237, v241, v242\n\t" \
  "v_fma_f32 v238, v238, v241, v242\n\t" \
  "v_fma_f32 v239, v239, v241, v242\n\t" \
  "v_cvt_pk_bf16_f32 v72, v232, v233\n\t" \
  "v_cvt_pk_bf16_f32 v73, v234, v235\n\t" \
  "v_cvt_pk_bf16_f32 v74, v236, v237\n\t" \
  "v_cvt_pk_bf16_f32 v75, v238, v239\n\t" \
  "global_load_dwordx4 v[168:171], v253, %0 offset:2048 sc1\n\t" \
  "v_lshlrev_b32 v232, 16, v76\n\t" \
  "v_lshlrev_b32 v234, 16, v77\n\t" \
  "v_lshlrev_b32 v236, 16, v78\n\t" \
  "v_lshlrev_b32 v238, 16, v79\n\t" \
  "v_and_b32 v233, 0xffff0000, v76\n\t" \
  "v_and_b32 v235, 0xffff0000, v77\n\t" \
  "v_and_b32 v237, 0xffff0000, v78\n\t" \
  "v_and_b32 v239, 0xffff0000, v79\n\t" \
  "v_fma_f32 v232, v232, v241, v242\n\t" \
  "v_fma_f32 v233, v233, v241, v242\n\t" \
  "v_fma_f32 v234, v234, v241, v242\n\t" \
  "v_fma_f32 v235, v235, v241, v242\n\t" \
  "v_fma_f32 v236, v236, v241, v242\n\t" \
  "v_fma_f32 v237, v237, v241, v242\n\t" \
  "v_fma_f32 v238, v238, v241, v242\n\t" \
  "v_fma_f32 v239, v239, v241, v242\n\t" \
  "v_cvt_pk_bf16_f32 v76, v232, v233\n\t" \
  "v_cvt_pk_bf16_f32 v77, v234, v235\n\t" \
  "v_cvt_pk_bf16_f32 v78, v236, v237\n\t" \
  "v_cvt_pk_bf16_f32 v79, v238, v239\n\t" \
  "global_load_dwordx4 v[172:175], v253, %0 offset:3072 sc1\n\t" \
  "s_waitcnt vmcnt(22)\n\t" \
  "v_lshlrev_b32 v232, 16, v80\n\t" \
  "v_lshlrev_b32 v234, 16, v81\n\t" \
  "v_lshlrev_b32 v236, 16, v82\n\t" \
  "v_lshlrev_b32 v238, 16, v83\n\t" \
  "v_and_b32 v233, 0xffff0000, v80\n\t" \
  "v_and_b32 v235, 0xffff0000, v81\n\t" \
  "v_and_b32 v237, 0xffff0000, v82\n\t" \
  "v_and_b32 v239, 0xffff0000, v83\n\t" \
  "v_fma_f32 v232, v232, v241, v242\n\t" \
  "v_fma_f32 v233, v233, v241, v242\n\t" \
  "v_fma_f32 v234, v234, v241, v242\n\t" \
  "v_fma_f32 v235, v235, v241, v242\n\t" \
  "v_fma_f32 v236, v236, v241, v242\n\t" \
  "v_fma_f32 v237, v237, v241, v242\n\t" \
  "v_fma_f32 v238, v238, v241, v242\n\t" \
  "v_fma_f32 v239, v239, v241, v242\n\t" \
  "v_cvt_pk_bf16_f32 v80, v232, v233\n\t" \
  "v_cvt_pk_bf16_f32 v81, v234, v235\n\t" \
  "v_cvt_pk_bf16_f32 v82, v236, v237\n\t" \
  "v_cvt_pk_bf16_f32 v83, v238, v239\n\t" \
  "s_add_u32 m0, s54, 0\n\t" \
  "s_add_u32 s57, s52, 0\n\t" \
  "buffer_load_dwordx4 v249, s[40:43], s57 offen lds\n\t" \
  "v_lshlrev_b32 v232, 16, v84\n\t" \
  "v_lshlrev_b32 v234, 16, v85\n\t" \
  "v_lshlrev_b32 v236, 16, v86\n\t" \
  "v_lshlrev_b32 v238, 16, v87\n\t" \
  "v_and_b32 v233, 0xffff0000, v84\n\t" \
  "v_and_b32 v235, 0xffff0000, v85\n\t" \
  "v_and_b32 v237, 0xffff0000, v86\n\t" \
  "v_and_b32 v239, 0xffff0000, v87\n\t" \
  "v_fma_f32 v232, v232, v241, v242\n\t" \
  "v_fma_f32 v233, v233, v241, v242\n\t" \
  "v_fma_f32 v234, v234, v241, v242\n\t" \
  "v_fma_f32 v235, v235, v241, v242\n\t" \
  "v_fma_f32 v236, v236, v241, v242\n\t" \
  "v_fma_f32 v237, v237, v241, v242\n\t" \
  "v_fma_f32 v238, v238, v241, v242\n\t" \
  "v_fma_f32 v239, v239, v241, v242\n\t" \
  "v_cvt_pk_bf16_f32 v84, v232, v233\n\t" \
  "v_cvt_pk_bf16_f32 v85, v234, v235\n\t" \
  "v_cvt_pk_bf16_f32 v86, v236, v237\n\t" \
  "v_cvt_pk_bf16_f32 v87, v238, v239\n\t" \
  "s_add_u32 m0, s54, 1024\n\t" \
  "s_add_u32 s57, s52, 73728\n\t" \
  "buffer_load_dwordx4 v249, s[40:43], s57 offen lds\n\t" \
  "s_waitcnt vmcnt(22)\n\t" \
  "v_lshlrev_b32 v232, 16, v88\n\t" \
  "v_lshlrev_b32 v234, 16, v89\n\t" \
  "v_lshlrev_b32 v236, 16, v90\n\t" \
  "v_lshlrev_b32 v238, 16, v91\n\t" \
  "v_and_b32 v233, 0xffff0000, v88\n\t" \
  "v_and_b32 v235, 0xffff0000, v89\n\t" \
  "v_and_b32 v237, 0xffff0000, v90\n\t" \
  "v_and_b32 v239, 0xffff0000, v91\n\t" \
  "v_fma_f32 v232, v232, v241, v242\n\t" \
  "v_fma_f32 v233, v233, v241, v242\n\t" \
  "v_fma_f32 v234, v234, v241, v242\n\t" \
  "v_fma_f32 v235, v235, v241, v242\n\t" \
  "v_fma_f32 v236, v236, v241, v242\n\t" \
  "v_fma_f32 v237, v237, v241, v242\n\t" \
  "v_fma_f32 v238, v238, v241, v242\n\t" \
  "v_fma_f32 v239, v239, v241, v242\n\t" \
  "v_cvt_pk_bf16_f32 v88, v232, v233\n\t" \
  "v_cvt_pk_bf16_f32 v89, v234, v235\n\t" \
  "v_cvt_pk_bf16_f32 v90, v236, v237\n\t" \
  "v_cvt_pk_bf16_f32 v91, v238, v239\n\t" \
  "s_add_u32 m0, s54, 2048\n\t" \
  "s_add_u32 s57, s52, 147456\n\t" \
  "buffer_load_dwordx4 v249, s[40:43], s57 offen lds\n\t" \
  "v_lshlrev_b32 v232, 16, v92\n\t" \
  "v_lshlrev_b32 v234, 16, v93\n\t" \
  "v_lshlrev_b32 v236, 16, v94\n\t" \
  "v_lshlrev_b32 v238, 16, v95\n\t" \
  "v_and_b32 v233, 0xffff0000, v92\n\t" \
  "v_and_b32 v235, 0xffff0000, v93\n\t" \
  "v_and_b32 v237, 0xffff0000, v94\n\t" \
  "v_and_b32 v239, 0xffff0000, v95\n\t" \
  "v_fma_f32 v232, v232, v241, v242\n\t" \
  "v_fma_f32 v233, v233, v241, v242\n\t" \
  "v_fma_f32 v234, v234, v241, v242\n\t" \
  "v_fma_f32 v235, v235, v241, v242\n\t" \
  "v_fma_f32 v236, v236, v241, v242\n\t" \
  "v_fma_f32 v237, v237, v241, v242\n\t" \
  "v_fma_f32 v238, v238, v241, v242\n\t" \
  "v_fma_f32 v239, v239, v241, v242\n\t" \
  "v_cvt_pk_bf16_f32 v92, v232, v233\n\t" \
  "v_cvt_pk_bf16_f32 v93, v234, v235\n\t" \
  "v_cvt_pk_bf16_f32 v94, v236, v237\n\t" \
  "v_cvt_pk_bf16_f32 v95, v238, v239\n\t" \
  "s_add_u32 m0, s54, 3072\n\t" \
  "s_add_u32 s57, s52, 221184\n\t" \
  "buffer_load_dwordx4 v249, s[40:43], s57 offen lds\n\t" \
  "s_waitcnt vmcnt(22)\n\t" \
  "v_lshlrev_b32 v232, 16, v96\n\t" \
  "v_lshlrev_b32 v234, 16, v97\n\t" \
  "v_lshlrev_b32 v236, 16, v98\n\t" \
  "v_lshlrev_b32 v238, 16, v99\n\t" \
  "v_and_b32 v233, 0xffff0000, v96\n\t" \
  "v_and_b32 v235, 0xffff0000, v97\n\t" \
  "v_and_b32 v237, 0xffff0000, v98\n\t" \
  "v_and_b32 v239, 0xffff0000, v99\n\t" \
  "v_fma_f32 v232, v232, v241, v242\n\t" \
  "v_fma_f32 v233, v233, v241, v242\n\t" \
  "v_fma_f32 v234, v234, v241, v242\n\t" \
  "v_fma_f32 v235, v235, v241, v242\n\t" \
  "v_fma_f32 v236, v236, v241, v242\n\t" \
  "v_fma_f32 v237, v237, v241, v242\n\t" \
  "v_fma_f32 v238, v238, v241, v242\n\t" \
  "v_fma_f32 v239, v239, v241, v242\n\t" \
  "v_cvt_pk_bf16_f32 v96, v232, v233\n\t" \
  "v_cvt_pk_bf16_f32 v97, v234, v235\n\t" \
  "v_cvt_pk_bf16_f32 v98, v236, v237\n\t" \
  "v_cvt_pk_bf16_f32 v99, v238, v239\n\t" \
  "s_add_u32 m0, s54, 4096\n\t" \
  "s_add_u32 s57, s52, 294912\n\t" \
  "buffer_load_dwordx4 v249, s[40:43], s57 offen lds\n\t" \
  "v_lshlrev_b32 v232, 16, v100\n\t" \
  "v_lshlrev_b32 v234, 16, v101\n\t" \
  "v_lshlrev_b32 v236, 16, v102\n\t" \
  "v_lshlrev_b32 v238, 16, v103\n\t" \
  "v_and_b32 v233, 0xffff0000, v100\n\t" \
  "v_and_b32 v235, 0xffff0000, v101\n\t" \
  "v_and_b32 v237, 0xffff0000, v102\n\t" \
  "v_and_b32 v239, 0xffff0000, v103\n\t" \
  "v_fma_f32 v232, v232, v241, v242\n\t" \
  "v_fma_f32 v233, v233, v241, v242\n\t" \
  "v_fma_f32 v234, v234, v241, v242\n\t" \
  "v_fma_f32 v235, v235, v241, v242\n\t" \
  "v_fma_f32 v236, v236, v241, v242\n\t" \
  "v_fma_f32 v237, v237, v241, v242\n\t" \
  "v_fma_f32 v238, v238, v241, v242\n\t" \
  "v_fma_f32 v239, v239, v241, v242\n\t" \
  "v_cvt_pk_bf16_f32 v100, v232, v233\n\t" \
  "v_cvt_pk_bf16_f32 v101, v234, v235\n\t" \
  "v_cvt_pk_bf16_f32 v102, v236, v237\n\t" \
  "v_cvt_pk_bf16_f32 v103, v238, v239\n\t" \
  "s_add_u32 m0, s54, 5120\n\t" \
  "s_add_u32 s57, s52, 368640\n\t" \
  "buffer_load_dwordx4 v249, s[40:43], s57 offen lds\n\t" \
  "s_waitcnt vmcnt(22)\n\t" \
  "v_lshlrev_b32 v232, 16, v104\n\t" \
  "v_lshlrev_b32 v234, 16, v105\n\t" \
  "v_lshlrev_b32 v236, 16, v106\n\t" \
  "v_lshlrev_b32 v238, 16, v107\n\t" \
  "v_and_b32 v233, 0xffff0000, v104\n\t" \
  "v_and_b32 v235, 0xffff0000, v105\n\t" \
  "v_and_b32 v237, 0xffff0000, v106\n\t" \
  "v_and_b32 v239, 0xffff0000, v107\n\t" \
  "v_fma_f32 v232, v232, v241, v242\n\t" \
  "v_fma_f32 v233, v233, v241, v242\n\t" \
  "v_fma_f32 v234, v234, v241, v242\n\t" \
  "v_fma_f32 v235, v235, v241, v242\n\t" \
  "v_fma_f32 v236, v236, v241, v242\n\t" \
  "v_fma_f32 v237, v237, v241, v242\n\t" \
  "v_fma_f32 v238, v238, v241, v242\n\t" \
  "v_fma_f32 v239, v239, v241, v242\n\t" \
  "v_cvt_pk_bf16_f32 v104, v232, v233\n\t" \
  "v_cvt_pk_bf16_f32 v105, v234, v235\n\t" \
  "v_cvt_pk_bf16_f32 v106, v236, v237\n\t" \
  "v_cvt_pk_bf16_f32 v107, v238, v239\n\t" \
  "s_add_u32 m0, s54, 6144\n\t" \
  "s_add_u32 s57, s52, 442368\n\t" \
  "buffer_load_dwordx4 v249, s[40:43], s57 offen lds\n\t" \
  "v_lshlrev_b32 v232, 16, v108\n\t" \
  "v_lshlrev_b32 v234, 16, v109\n\t" \
  "v_lshlrev_b32 v236, 16, v110\n\t" \
  "v_lshlrev_b32 v238, 16, v111\n\t" \
  "v_and_b32 v233, 0xffff0000, v108\n\t" \
  "v_and_b32 v235, 0xffff0000, v109\n\t" \
  "v_and_b32 v237, 0xffff0000, v110\n\t" \
  "v_and_b32 v239, 0xffff0000, v111\n\t" \
  "v_fma_f32 v232, v232, v241, v242\n\t" \
  "v_fma_f32 v233, v233, v241, v242\n\t" \
  "v_fma_f32 v234, v234, v241, v242\n\t" \
  "v_fma_f32 v235, v235, v241, v242\n\t" \
  "v_fma_f32 v236, v236, v241, v242\n\t" \
  "v_fma_f32 v237, v237, v241, v242\n\t" \
  "v_fma_f32 v238, v238, v241, v242\n\t" \
  "v_fma_f32 v239, v239, v241, v242\n\t" \
  "v_cvt_pk_bf16_f32 v108, v232, v233\n\t" \
  "v_cvt_pk_bf16_f32 v109, v234, v235\n\t" \
  "v_cvt_pk_bf16_f32 v110, v236, v237\n\t" \
  "v_cvt_pk_bf16_f32 v111, v238, v239\n\t" \
  "s_add_u32 m0, s54, 7168\n\t" \
  "s_add_u32 s57, s52, 516096\n\t" \
  "buffer_load_dwordx4 v249, s[40:43], s57 offen lds\n\t" \
  "s_waitcnt vmcnt(22)\n\t" \
  "v_lshlrev_b32 v232, 16, v112\n\t" \
  "v_lshlrev_b32 v234, 16, v113\n\t" \
  "v_lshlrev_b32 v236, 16, v114\n\t" \
  "v_lshlrev_b32 v238, 16, v115\n\t" \
  "v_and_b32 v233, 0xffff0000, v112\n\t" \
  "v_and_b32 v235, 0xffff0000, v113\n\t" \
  "v_and_b32 v237, 0xffff0000, v114\n\t" \
  "v_and_b32 v239, 0xffff0000, v115\n\t" \
  "v_fma_f32 v232, v232, v241, v242\n\t" \
  "v_fma_f32 v233, v233, v241, v242\n\t" \
  "v_fma_f32 v234, v234, v241, v242\n\t" \
  "v_fma_f32 v235, v235, v241, v242\n\t" \
  "v_fma_f32 v236, v236, v241, v242\n\t" \
  "v_fma_f32 v237, v237, v241, v242\n\t" \
  "v_fma_f32 v238, v238, v241, v242\n\t" \
  "v_fma_f32 v239, v239, v241, v242\n\t" \
  "v_cvt_pk_bf16_f32 v112, v232, v233\n\t" \
  "v_cvt_pk_bf16_f32 v113, v234, v235\n\t" \
  "v_cvt_pk_bf16_f32 v114, v236, v237\n\t" \
  "v_cvt_pk_bf16_f32 v115, v238, v239\n\t" \
  "s_add_u32 m0, s54, 8192\n\t" \
  "s_add_u32 s57, s52, 589824\n\t" \
  "buffer_load_dwordx4 v249, s[40:43], s57 offen lds\n\t" \
  "v_lshlrev_b32 v232, 16, v116\n\t" \
  "v_lshlrev_b32 v234, 16, v117\n\t" \
  "v_lshlrev_b32 v236, 16, v118\n\t" \
  "v_lshlrev_b32 v238, 16, v119\n\t" \
  "v_and_b32 v233, 0xffff0000, v116\n\t" \
  "v_and_b32 v235, 0xffff0000, v117\n\t" \
  "v_and_b32 v237, 0xffff0000, v118\n\t" \
  "v_and_b32 v239, 0xffff0000, v119\n\t" \
  "v_fma_f32 v232, v232, v241, v242\n\t" \
  "v_fma_f32 v233, v233, v241, v242\n\t" \
  "v_fma_f32 v234, v234, v241, v242\n\t" \
  "v_fma_f32 v235, v235, v241, v242\n\t" \
  "v_fma_f32 v236, v236, v241, v242\n\t" \
  "v_fma_f32 v237, v237, v241, v242\n\t" \
  "v_fma_f32 v238, v238, v241, v242\n\t" \
  "v_fma_f32 v239, v239, v241, v242\n\t" \
  "v_cvt_pk_bf16_f32 v116, v232, v233\n\t" \
  "v_cvt_pk_bf16_f32 v117, v234, v235\n\t" \
  "v_cvt_pk_bf16_f32 v118, v236, v237\n\t" \
  "v_cvt_pk_bf16_f32 v119, v238, v239\n\t" \
  "s_add_u32 m0, s54, 36864\n\t" \
  "s_add_u32 s57, s52, 2654208\n\t" \
  "buffer_load_dwordx4 v249, s[40:43], s57 offen lds\n\t" \
  "s_waitcnt vmcnt(22)\n\t" \
  "v_lshlrev_b32 v232, 16, v120\n\t" \
  "v_lshlrev_b32 v234, 16, v121\n\t" \
  "v_lshlrev_b32 v236, 16, v122\n\t" \
  "v_lshlrev_b32 v238, 16, v123\n\t" \
  "v_and_b32 v233, 0xffff0000, v120\n\t" \
  "v_and_b32 v235, 0xffff0000, v121\n\t" \
  "v_and_b32 v237, 0xffff0000, v122\n\t" \
  "v_and_b32 v239, 0xffff0000, v123\n\t" \
  "v_fma_f32 v232, v232, v241, v242\n\t" \
  "v_fma_f32 v233, v233, v241, v242\n\t" \
  "v_fma_f32 v234, v234, v241, v242\n\t" \
  "v_fma_f32 v235, v235, v241, v242\n\t" \
  "v_fma_f32 v236, v236, v241, v242\n\t" \
  "v_fma_f32 v237, v237, v241, v242\n\t" \
  "v_fma_f32 v238, v238, v241, v242\n\t" \
  "v_fma_f32 v239, v239, v241, v242\n\t" \
  "v_cvt_pk_bf16_f32 v120, v232, v233\n\t" \
  "v_cvt_pk_bf16_f32 v121, v234, v235\n\t" \
  "v_cvt_pk_bf16_f32 v122, v236, v237\n\t" \
  "v_cvt_pk_bf16_f32 v123, v238, v239\n\t" \
  "s_add_u32 m0, s54, 37888\n\t" \
  "s_add_u32 s57, s52, 2727936\n\t" \
  "buffer_load_dwordx4 v249, s[40:43], s57 offen lds\n\t" \
  "v_lshlrev_b32 v232, 16, v124\n\t" \
  "v_lshlrev_b32 v234, 16, v125\n\t" \
  "v_lshlrev_b32 v236, 16, v126\n\t" \
  "v_lshlrev_b32 v238, 16, v127\n\t" \
  "v_and_b32 v233, 0xffff0000, v124\n\t" \
  "v_and_b32 v235, 0xffff0000, v125\n\t" \
  "v_and_b32 v237, 0xffff0000, v126\n\t" \
  "v_and_b32 v239, 0xffff0000, v127\n\t" \
  "v_fma_f32 v232, v232, v241, v242\n\t" \
  "v_fma_f32 v233, v233, v241, v242\n\t" \
  "v_fma_f32 v234, v234, v241, v242\n\t" \
  "v_fma_f32 v235, v235, v241, v242\n\t" \
  "v_fma_f32 v236, v236, v241, v242\n\t" \
  "v_fma_f32 v237, v237, v241, v242\n\t" \
  "v_fma_f32 v238, v238, v241, v242\n\t" \
  "v_fma_f32 v239, v239, v241, v242\n\t" \
  "v_cvt_pk_bf16_f32 v124, v232, v233\n\t" \
  "v_cvt_pk_bf16_f32 v125, v234, v235\n\t" \
  "v_cvt_pk_bf16_f32 v126, v236, v237\n\t" \
  "v_cvt_pk_bf16_f32 v127, v238, v239\n\t" \
  "s_add_u32 m0, s54, 38912\n\t" \
  "s_add_u32 s57, s52, 2801664\n\t" \
  "buffer_load_dwordx4 v249, s[40:43], s57 offen lds\n\t" \
  "s_waitcnt vmcnt(22)\n\t" \
  "v_lshlrev_b32 v232, 16, v128\n\t" \
  "v_lshlrev_b32 v234, 16, v129\n\t" \
  "v_lshlrev_b32 v236, 16, v130\n\t" \
  "v_lshlrev_b32 v238, 16, v131\n\t" \
  "v_and_b32 v233, 0xffff0000, v128\n\t" \
  "v_and_b32 v235, 0xffff0000, v129\n\t" \
  "v_and_b32 v237, 0xffff0000, v130\n\t" \
  "v_and_b32 v239, 0xffff0000, v131\n\t" \
  "v_fma_f32 v232, v232, v241, v242\n\t" \
  "v_fma_f32 v233, v233, v241, v242\n\t" \
  "v_fma_f32 v234, v234, v241, v242\n\t" \
  "v_fma_f32 v235, v235, v241, v242\n\t" \
  "v_fma_f32 v236, v236, v241, v242\n\t" \
  "v_fma_f32 v237, v237, v241, v242\n\t" \
  "v_fma_f32 v238, v238, v241, v242\n\t" \
  "v_fma_f32 v239, v239, v241, v242\n\t" \
  "v_cvt_pk_bf16_f32 v128, v232, v233\n\t" \
  "v_cvt_pk_bf16_f32 v129, v234, v235\n\t" \
  "v_cvt_pk_bf16_f32 v130, v236, v237\n\t" \
  "v_cvt_pk_bf16_f32 v131, v238, v239\n\t" \
  "s_add_u32 m0, s54, 39936\n\t" \
  "s_add_u32 s57, s52, 2875392\n\t" \
  "buffer_load_dwordx4 v249, s[40:43], s57 offen lds\n\t" \
  "v_lshlrev_b32 v232, 16, v132\n\t" \
  "v_lshlrev_b32 v234, 16, v133\n\t" \
  "v_lshlrev_b32 v236, 16, v134\n\t" \
  "v_lshlrev_b32 v238, 16, v135\n\t" \
  "v_and_b32 v233, 0xffff0000, v132\n\t" \
  "v_and_b32 v235, 0xffff0000, v133\n\t" \
  "v_and_b32 v237, 0xffff0000, v134\n\t" \
  "v_and_b32 v239, 0xffff0000, v135\n\t" \
  "v_fma_f32 v232, v232, v241, v242\n\t" \
  "v_fma_f32 v233, v233, v241, v242\n\t" \
  "v_fma_f32 v234, v234, v241, v242\n\t" \
  "v_fma_f32 v235, v235, v241, v242\n\t" \
  "v_fma_f32 v236, v236, v241, v242\n\t" \
  "v_fma_f32 v237, v237, v241, v242\n\t" \
  "v_fma_f32 v238, v238, v241, v242\n\t" \
  "v_fma_f32 v239, v239, v241, v242\n\t" \
  "v_cvt_pk_bf16_f32 v132, v232, v233\n\t" \
  "v_cvt_pk_bf16_f32 v133, v234, v235\n\t" \
  "v_cvt_pk_bf16_f32 v134, v236, v237\n\t" \
  "v_cvt_pk_bf16_f32 v135, v238, v239\n\t" \
  "s_add_u32 m0, s54, 40960\n\t" \
  "s_add_u32 s57, s52, 2949120\n\t" \
  "buffer_load_dwordx4 v249, s[40:43], s57 offen lds\n\t" \
  "s_waitcnt vmcnt(22)\n\t" \
  "v_lshlrev_b32 v232, 16, v136\n\t" \
  "v_lshlrev_b32 v234, 16, v137\n\t" \
  "v_lshlrev_b32 v236, 16, v138\n\t" \
  "v_lshlrev_b32 v238, 16, v139\n\t" \
  "v_and_b32 v233, 0xffff0000, v136\n\t" \
  "v_and_b32 v235, 0xffff0000, v137\n\t" \
  "v_and_b32 v237, 0xffff0000, v138\n\t" \
  "v_and_b32 v239, 0xffff0000, v139\n\t" \
  "v_fma_f32 v232, v232, v241, v242\n\t" \
  "v_fma_f32 v233, v233, v241, v242\n\t" \
  "v_fma_f32 v234, v234, v241, v242\n\t" \
  "v_fma_f32 v235, v235, v241, v242\n\t" \
  "v_fma_f32 v236, v236, v241, v242\n\t" \
  "v_fma_f32 v237, v237, v241, v242\n\t" \
  "v_fma_f32 v238, v238, v241, v242\n\t" \
  "v_fma_f32 v239, v239, v241, v242\n\t" \
  "v_cvt_pk_bf16_f32 v136, v232, v233\n\t" \
  "v_cvt_pk_bf16_f32 v137, v234, v235\n\t" \
  "v_cvt_pk_bf16_f32 v138, v236, v237\n\t" \
  "v_cvt_pk_bf16_f32 v139, v238, v239\n\t" \
  "s_add_u32 m0, s54, 41984\n\t" \
  "s_add_u32 s57, s52, 3022848\n\t" \
  "buffer_load_dwordx4 v249, s[40:43], s57 offen lds\n\t" \
  "v_lshlrev_b32 v232, 16, v140\n\t" \
  "v_lshlrev_b32 v234, 16, v141\n\t" \
  "v_lshlrev_b32 v236, 16, v142\n\t" \
  "v_lshlrev_b32 v238, 16, v143\n\t" \
  "v_and_b32 v233, 0xffff0000, v140\n\t" \
  "v_and_b32 v235, 0xffff0000, v141\n\t" \
  "v_and_b32 v237, 0xffff0000, v142\n\t" \
  "v_and_b32 v239, 0xffff0000, v143\n\t" \
  "v_fma_f32 v232, v232, v241, v242\n\t" \
  "v_fma_f32 v233, v233, v241, v242\n\t" \
  "v_fma_f32 v234, v234, v241, v242\n\t" \
  "v_fma_f32 v235, v235, v241, v242\n\t" \
  "v_fma_f32 v236, v236, v241, v242\n\t" \
  "v_fma_f32 v237, v237, v241, v242\n\t" \
  "v_fma_f32 v238, v238, v241, v242\n\t" \
  "v_fma_f32 v239, v239, v241, v242\n\t" \
  "v_cvt_pk_bf16_f32 v140, v232, v233\n\t" \
  "v_cvt_pk_bf16_f32 v141, v234, v235\n\t" \
  "v_cvt_pk_bf16_f32 v142, v236, v237\n\t" \
  "v_cvt_pk_bf16_f32 v143, v238, v239\n\t" \
  "s_add_u32 m0, s54, 43008\n\t" \
  "s_add_u32 s57, s52, 3096576\n\t" \
  "buffer_load_dwordx4 v249, s[40:43], s57 offen lds\n\t" \
  "s_waitcnt vmcnt(22)\n\t" \
  "v_lshlrev_b32 v232, 16, v144\n\t" \
  "v_lshlrev_b32 v234, 16, v145\n\t" \
  "v_lshlrev_b32 v236, 16, v146\n\t" \
  "v_lshlrev_b32 v238, 16, v147\n\t" \
  "v_and_b32 v233, 0xffff0000, v144\n\t" \
  "v_and_b32 v235, 0xffff0000, v145\n\t" \
  "v_and_b32 v237, 0xffff0000, v146\n\t" \
  "v_and_b32 v239, 0xffff0000, v147\n\t" \
  "v_fma_f32 v232, v232, v241, v242\n\t" \
  "v_fma_f32 v233, v233, v241, v242\n\t" \
  "v_fma_f32 v234, v234, v241, v242\n\t" \
  "v_fma_f32 v235, v235, v241, v242\n\t" \
  "v_fma_f32 v236, v236, v241, v242\n\t" \
  "v_fma_f32 v237, v237, v241, v242\n\t" \
  "v_fma_f32 v238, v238, v241, v242\n\t" \
  "v_fma_f32 v239, v239, v241, v242\n\t" \
  "v_cvt_pk_bf16_f32 v144, v232, v233\n\t" \
  "v_cvt_pk_bf16_f32 v145, v234, v235\n\t" \
  "v_cvt_pk_bf16_f32 v146, v236, v237\n\t" \
  "v_cvt_pk_bf16_f32 v147, v238, v239\n\t" \
  "s_add_u32 m0, s54, 44032\n\t" \
  "s_add_u32 s57, s52, 3170304\n\t" \
  "buffer_load_dwordx4 v249, s[40:43], s57 offen lds\n\t" \
  "v_lshlrev_b32 v232, 16, v148\n\t" \
  "v_lshlrev_b32 v234, 16, v149\n\t" \
  "v_lshlrev_b32 v236, 16, v150\n\t" \
  "v_lshlrev_b32 v238, 16, v151\n\t" \
  "v_and_b32 v233, 0xffff0000, v148\n\t" \
  "v_and_b32 v235, 0xffff0000, v149\n\t" \
  "v_and_b32 v237, 0xffff0000, v150\n\t" \
  "v_and_b32 v239, 0xffff0000, v151\n\t" \
  "v_fma_f32 v232, v232, v241, v242\n\t" \
  "v_fma_f32 v233, v233, v241, v242\n\t" \
  "v_fma_f32 v234, v234, v241, v242\n\t" \
  "v_fma_f32 v235, v235, v241, v242\n\t" \
  "v_fma_f32 v236, v236, v241, v242\n\t" \
  "v_fma_f32 v237, v237, v241, v242\n\t" \
  "v_fma_f32 v238, v238, v241, v242\n\t" \
  "v_fma_f32 v239, v239, v241, v242\n\t" \
  "v_cvt_pk_bf16_f32 v148, v232, v233\n\t" \
  "v_cvt_pk_bf16_f32 v149, v234, v235\n\t" \
  "v_cvt_pk_bf16_f32 v150, v236, v237\n\t" \
  "v_cvt_pk_bf16_f32 v151, v238, v239\n\t" \
  "s_add_u32 m0, s54, 45056\n\t" \
  "s_add_u32 s57, s52, 3244032\n\t" \
  "buffer_load_dwordx4 v249, s[40:43], s57 offen lds\n\t" \
  "s_waitcnt vmcnt(22)\n\t" \
  "v_lshlrev_b32 v232, 16, v152\n\t" \
  "v_lshlrev_b32 v234, 16, v153\n\t" \
  "v_lshlrev_b32 v236, 16, v154\n\t" \
  "v_lshlrev_b32 v238, 16, v155\n\t" \
  "v_and_b32 v233, 0xffff0000, v152\n\t" \
  "v_and_b32 v235, 0xffff0000, v153\n\t" \
  "v_and_b32 v237, 0xffff0000, v154\n\t" \
  "v_and_b32 v239, 0xffff0000, v155\n\t" \
  "v_fma_f32 v232, v232, v241, v242\n\t" \
  "v_fma_f32 v233, v233, v241, v242\n\t" \
  "v_fma_f32 v234, v234, v241, v242\n\t" \
  "v_fma_f32 v235, v235, v241, v242\n\t" \
  "v_fma_f32 v236, v236, v241, v242\n\t" \
  "v_fma_f32 v237, v237, v241, v242\n\t" \
  "v_fma_f32 v238, v238, v241, v242\n\t" \
  "v_fma_f32 v239, v239, v241, v242\n\t" \
  "v_cvt_pk_bf16_f32 v152, v232, v233\n\t" \
  "v_cvt_pk_bf16_f32 v153, v234, v235\n\t" \
  "v_cvt_pk_bf16_f32 v154, v236, v237\n\t" \
  "v_cvt_pk_bf16_f32 v155, v238, v239\n\t" \
  "s_add_u32 m0, s55, 0\n\t" \
  "s_add_u32 s57, s53, 0\n\t" \
  "buffer_load_dwordx4 v250, s[44:47], s57 offen lds\n\t" \
  "v_lshlrev_b32 v232, 16, v156\n\t" \
  "v_lshlrev_b32 v234, 16, v157\n\t" \
  "v_lshlrev_b32 v236, 16, v158\n\t" \
  "v_lshlrev_b32 v238, 16, v159\n\t" \
  "v_and_b32 v233, 0xffff0000, v156\n\t" \
  "v_and_b32 v235, 0xffff0000, v157\n\t" \
  "v_and_b32 v237, 0xffff0000, v158\n\t" \
  "v_and_b32 v239, 0xffff0000, v159\n\t" \
  "v_fma_f32 v232, v232, v241, v242\n\t" \
  "v_fma_f32 v233, v233, v241, v242\n\t" \
  "v_fma_f32 v234, v234, v241, v242\n\t" \
  "v_fma_f32 v235, v235, v241, v242\n\t" \
  "v_fma_f32 v236, v236, v241, v242\n\t" \
  "v_fma_f32 v237, v237, v241, v242\n\t" \
  "v_fma_f32 v238, v238, v241, v242\n\t" \
  "v_fma_f32 v239, v239, v241, v242\n\t" \
  "v_cvt_pk_bf16_f32 v156, v232, v233\n\t" \
  "v_cvt_pk_bf16_f32 v157, v234, v235\n\t" \
  "v_cvt_pk_bf16_f32 v158, v236, v237\n\t" \
  "v_cvt_pk_bf16_f32 v159, v238, v239\n\t" \
  "s_add_u32 m0, s55, 1088\n\t" \
  "s_add_u32 s57, s53, 1152\n\t" \
  "buffer_load_dwordx4 v250, s[44:47], s57 offen lds\n\t" \
  "s_waitcnt vmcnt(22)\n\t" \
  "v_lshlrev_b32 v232, 16, v160\n\t" \
  "v_lshlrev_b32 v234, 16, v161\n\t" \
  "v_lshlrev_b32 v236, 16, v162\n\t" \
  "v_lshlrev_b32 v238, 16, v163\n\t" \
  "v_and_b32 v233, 0xffff0000, v160\n\t" \
  "v_and_b32 v235, 0xffff0000, v161\n\t" \
  "v_and_b32 v237, 0xffff0000, v162\n\t" \
  "v_and_b32 v239, 0xffff0000, v163\n\t" \
  "v_fma_f32 v232, v232, v241, v242\n\t" \
  "v_fma_f32 v233, v233, v241, v242\n\t" \
  "v_fma_f32 v234, v234, v241, v242\n\t" \
  "v_fma_f32 v235, v235, v241, v242\n\t" \
  "v_fma_f32 v236, v236, v241, v242\n\t" \
  "v_fma_f32 v237, v237, v241, v242\n\t" \
  "v_fma_f32 v238, v238, v241, v242\n\t" \
  "v_fma_f32 v239, v239, v241, v242\n\t" \
  "v_cvt_pk_bf16_f32 v160, v232, v233\n\t" \
  "v_cvt_pk_bf16_f32 v161, v234, v235\n\t" \
  "v_cvt_pk_bf16_f32 v162, v236, v237\n\t" \
  "v_cvt_pk_bf16_f32 v163, v238, v239\n\t" \
  "s_add_u32 m0, s55, 2176\n\t" \
  "s_add_u32 s57, s53, 2304\n\t" \
  "buffer_load_dwordx4 v250, s[44:47], s57 offen lds\n\t" \
  "v_lshlrev_b32 v232, 16, v164\n\t" \
  "v_lshlrev_b32 v234, 16, v165\n\t" \
  "v_lshlrev_b32 v236, 16, v166\n\t" \
  "v_lshlrev_b32 v238, 16, v167\n\t" \
  "v_and_b32 v233, 0xffff0000, v164\n\t" \
  "v_and_b32 v235, 0xffff0000, v165\n\t" \
  "v_and_b32 v237, 0xffff0000, v166\n\t" \
  "v_and_b32 v239, 0xffff0000, v167\n\t" \
  "v_fma_f32 v232, v232, v241, v242\n\t" \
  "v_fma_f32 v233, v233, v241, v242\n\t" \
  "v_fma_f32 v234, v234, v241, v242\n\t" \
  "v_fma_f32 v235, v235, v241, v242\n\t" \
  "v_fma_f32 v236, v236, v241, v242\n\t" \
  "v_fma_f32 v237, v237, v241, v242\n\t" \
  "v_fma_f32 v238, v238, v241, v242\n\t" \
  "v_fma_f32 v239, v239, v241, v242\n\t" \
  "v_cvt_pk_bf16_f32 v164, v232, v233\n\t" \
  "v_cvt_pk_bf16_f32 v165, v234, v235\n\t" \
  "v_cvt_pk_bf16_f32 v166, v236, v237\n\t" \
  "v_cvt_pk_bf16_f32 v167, v238, v239\n\t" \
  "s_add_u32 m0, s55, 3264\n\t" \
  "s_add_u32 s57, s53, 3456\n\t" \
  "buffer_load_dwordx4 v250, s[44:47], s57 offen lds\n\t" \
  "s_waitcnt vmcnt(22)\n\t" \
  "v_lshlrev_b32 v232, 16, v168\n\t" \
  "v_lshlrev_b32 v234, 16, v169\n\t" \
  "v_lshlrev_b32 v236, 16, v170\n\t" \
  "v_lshlrev_b32 v238, 16, v171\n\t" \
  "v_and_b32 v233, 0xffff0000, v168\n\t" \
  "v_and_b32 v235, 0xffff0000, v169\n\t" \
  "v_and_b32 v237, 0xffff0000, v170\n\t" \
  "v_and_b32 v239, 0xffff0000, v171\n\t" \
  "v_fma_f32 v232, v232, v241, v242\n\t" \
  "v_fma_f32 v233, v233, v241, v242\n\t" \
  "v_fma_f32 v234, v234, v241, v242\n\t" \
  "v_fma_f32 v235, v235, v241, v242\n\t" \
  "v_fma_f32 v236, v236, v241, v242\n\t" \
  "v_fma_f32 v237, v237, v241, v242\n\t" \
  "v_fma_f32 v238, v238, v241, v242\n\t" \
  "v_fma_f32 v239, v239, v241, v242\n\t" \
  "v_cvt_pk_bf16_f32 v168, v232, v233\n\t" \
  "v_cvt_pk_bf16_f32 v169, v234, v235\n\t" \
  "v_cvt_pk_bf16_f32 v170, v236, v237\n\t" \
  "v_cvt_pk_bf16_f32 v171, v238, v239\n\t" \
  "s_add_u32 m0, s55, 4352\n\t" \
  "s_add_u32 s57, s53, 4608\n\t" \
  "buffer_load_dwordx4 v250, s[44:47], s57 offen lds\n\t" \
  "v_lshlrev_b32 v232, 16, v172\n\t" \
  "v_lshlrev_b32 v234, 16, v173\n\t" \
  "v_lshlrev_b32 v236, 16, v174\n\t" \
  "v_lshlrev_b32 v238, 16, v175\n\t" \
  "v_and_b32 v233, 0xffff0000, v172\n\t" \
  "v_and_b32 v235, 0xffff0000, v173\n\t" \
  "v_and_b32 v237, 0xffff0000, v174\n\t" \
  "v_and_b32 v239, 0xffff0000, v175\n\t" \
  "v_fma_f32 v232, v232, v241, v242\n\t" \
  "v_fma_f32 v233, v233, v241, v242\n\t" \
  "v_fma_f32 v234, v234, v241, v242\n\t" \
  "v_fma_f32 v235, v235, v241, v242\n\t" \
  "v_fma_f32 v236, v236, v241, v242\n\t" \
  "v_fma_f32 v237, v237, v241, v242\n\t" \
  "v_fma_f32 v238, v238, v241, v242\n\t" \
  "v_fma_f32 v239, v239, v241, v242\n\t" \
  "v_cvt_pk_bf16_f32 v172, v232, v233\n\t" \
  "v_cvt_pk_bf16_f32 v173, v234, v235\n\t" \
  "v_cvt_pk_bf16_f32 v174, v236, v237\n\t" \
  "v_cvt_pk_bf16_f32 v175, v238, v239\n\t" \
  "s_add_u32 m0, s55, 5440\n\t" \
  "s_add_u32 s57, s53, 5760\n\t" \
  "buffer_load_dwordx4 v250, s[44:47], s57 offen lds\n\t" \
  "s_add_u32 m0, s55, 6528\n\t" \
  "s_add_u32 s57, s53, 6912\n\t" \
  "buffer_load_dwordx4 v250, s[44:47], s57 offen lds\n\t" \
  "s_add_u32 m0, s55, 7616\n\t" \
  "s_add_u32 s57, s53, 8064\n\t" \
  "buffer_load_dwordx4 v250, s[44:47], s57 offen lds\n\t" \
  "s_add_u32 m0, s56, 0\n\t" \
  "s_add_u32 s57, s53, 0\n\t" \
  "buffer_load_dwordx4 v251, s[44:47], s57 offen lds\n\t" \
  "s_add_u32 m0, s55, 34816\n\t" \
  "s_add_u32 s57, s53, 36864\n\t" \
  "buffer_load_dwordx4 v250, s[44:47], s57 offen lds\n\t" \
  "s_add_u32 s52, s52, 128\n\t" \
  "s_waitcnt vmcnt(10)\n\t" \
  "s_barrier\n\t" \
  "ds_read_b64_tr_b16 v[208:209], v245 offset:0\n\t" \
  "ds_read_b64_tr_b16 v[210:211], v245 offset:512\n\t" \
  "ds_read_b64_tr_b16 v[212:213], v245 offset:2048\n\t" \
  "ds_read_b64_tr_b16 v[214:215], v245 offset:2560\n\t" \
  "ds_read_b64_tr_b16 v[216:217], v245 offset:4096\n\t" \
  "ds_read_b64_tr_b16 v[218:219], v245 offset:4608\n\t" \
  "ds_read_b64_tr_b16 v[220:221], v245 offset:6144\n\t" \
  "ds_read_b64_tr_b16 v[222:223], v245 offset:6656\n\t" \
  "ds_read_b64_tr_b16 v[224:225], v245 offset:8192\n\t" \
  "ds_read_b64_tr_b16 v[226:227], v245 offset:8704\n\t" \
  "ds_read_b64_tr_b16 v[228:229], v245 offset:10240\n\t" \
  "ds_read_b64_tr_b16 v[230:231], v245 offset:10752\n\t" \
  "s_mov_b32 s61, -1024\n\t" \
  "s_mov_b32 s62, -589824\n\t" \
  "s_mov_b32 s59, 9\n\t" \
  "MLP_LOOP_%=:\n\t" \
  "s_waitcnt lgkmcnt(6)\n\t" \
  "v_mfma_f32_32x32x16_bf16 v[176:191], v[208:211], v[32:35], 0\n\t" \
  "ds_read_b64_tr_b16 v[208:209], v245 offset:12288\n\t" \
  "ds_read_b64_tr_b16 v[210:211], v245 offset:12800\n\t" \
  "v_mfma_f32_32x32x16_bf16 v[176:191], v[212:215], v[36:39], v[176:191]\n\t" \
  "ds_read_b64_tr_b16 v[212:213], v245 offset:14336\n\t" \
  "ds_read_b64_tr_b16 v[214:215], v245 offset:14848\n\t" \
  "s_add_u32 m0, s55, 35904\n\t" \
  "s_add_u32 s57, s53, 38016\n\t" \
  "v_mfma_f32_32x32x16_bf16 v[176:191], v[216:219], v[40:43], v[176:191]\n\t" \
  "ds_read_b64_tr_b16 v[216:217], v245 offset:16384\n\t" \
  "ds_read_b64_tr_b16 v[218:219], v245 offset:16896\n\t" \
  "buffer_load_dwordx4 v250, s[44:47], s57 offen lds\n\t" \
  "s_waitcnt lgkmcnt(6)\n\t" \
  "v_mfma_f32_32x32x16_bf16 v[176:191], v[220:223], v[44:47], v[176:191]\n\t" \
  "ds_read_b64_tr_b16 v[220:221], v245 offset:18432\n\t" \
  "ds_read_b64_tr_b16 v[222:223], v245 offset:18944\n\t" \
  "v_mfma_f32_32x32x16_bf16 v[176:191], v[224:227], v[48:51], v[176:191]\n\t" \
  "ds_read_b64_tr_b16 v[224:225], v245 offset:20480\n\t" \
  "ds_read_b64_tr_b16 v[226:227], v245 offset:20992\n\t" \
  "s_add_u32 m0, s55, 36992\n\t" \
  "s_add_u32 s57, s53, 39168\n\t" \
  "v_mfma_f32_32x32x16_bf16 v[176:191], v[228:231], v[52:55], v[176:191]\n\t" \
  "ds_read_b64_tr_b16 v[228:229], v245 offset:22528\n\t" \
  "ds_read_b64_tr_b16 v[230:231], v245 offset:23040\n\t" \
  "buffer_load_dwordx4 v250, s[44:47], s57 offen lds\n\t" \
  "s_waitcnt lgkmcnt(6)\n\t" \
  "v_mfma_f32_32x32x16_bf16 v[176:191], v[208:211], v[56:59], v[176:191]\n\t" \
  "ds_read_b64_tr_b16 v[208:209], v245 offset:24576\n\t" \
  "ds_read_b64_tr_b16 v[210:211], v245 offset:25088\n\t" \
  "v_mfma_f32_32x32x16_bf16 v[176:191], v[212:215], v[60:63], v[176:191]\n\t" \
  "ds_read_b64_tr_b16 v[212:213], v245 offset:26624\n\t" \
  "ds_read_b64_tr_b16 v[214:215], v245 offset:27136\n\t" \
  "s_add_u32 m0, s55, 38080\n\t" \
  "s_add_u32 s57, s53, 40320\n\t" \
  "v_mfma_f32_32x32x16_bf16 v[176:191], v[216:219], v[64:67], v[176:191]\n\t" \
  "ds_read_b64_tr_b16 v[216:217], v245 offset:28672\n\t" \
  "ds_read_b64_tr_b16 v[218:219], v245 offset:29184\n\t" \
  "buffer_load_dwordx4 v250, s[44:47], s57 offen lds\n\t" \
  "s_waitcnt lgkmcnt(6)\n\t" \
  "v_mfma_f32_32x32x16_bf16 v[176:191], v[220:223], v[68:71], v[176:191]\n\t" \
  "ds_read_b64_tr_b16 v[220:221], v245 offset:30720\n\t" \
  "ds_read_b64_tr_b16 v[222:223], v245 offset:31232\n\t" \
  "v_mfma_f32_32x32x16_bf16 v[176:191], v[224:227], v[72:75], v[176:191]\n\t" \
  "ds_read_b64_tr_b16 v[224:225], v245 offset:32768\n\t" \
  "ds_read_b64_tr_b16 v[226:227], v245 offset:33280\n\t" \
  "s_add_u32 m0, s55, 39168\n\t" \
  "s_add_u32 s57, s53, 41472\n\t" \
  "v_mfma_f32_32x32x16_bf16 v[176:191], v[228:231], v[76:79], v[176:191]\n\t" \
  "ds_read_b64_tr_b16 v[228:229], v245 offset:34816\n\t" \
  "ds_read_b64_tr_b16 v[230:231], v245 offset:35328\n\t" \
  "buffer_load_dwordx4 v250, s[44:47], s57 offen lds\n\t" \
  "s_waitcnt lgkmcnt(6)\n\t" \
  "v_mfma_f32_32x32x16_bf16 v[176:191], v[208:211], v[80:83], v[176:191]\n\t" \
  "v_xor_b32 v245, 0x9000, v245\n\t" \
  "ds_read_b64_tr_b16 v[208:209], v245 offset:0\n\t" \
  "ds_read_b64_tr_b16 v[210:211], v245 offset:512\n\t" \
  "v_mfma_f32_32x32x16_bf16 v[176:191], v[212:215], v[84:87], v[176:191]\n\t" \
  "ds_read_b64_tr_b16 v[212:213], v245 offset:2048\n\t" \
  "ds_read_b64_tr_b16 v[214:215], v245 offset:2560\n\t" \
  "s_add_u32 m0, s55, 40256\n\t" \
  "s_add_u32 s57, s53, 42624\n\t" \
  "v_mfma_f32_32x32x16_bf16 v[176:191], v[216:219], v[88:91], v[176:191]\n\t" \
  "ds_read_b64_tr_b16 v[216:217], v245 offset:4096\n\t" \
  "ds_read_b64_tr_b16 v[218:219], v245 offset:4608\n\t" \
  "buffer_load_dwordx4 v250, s[44:47], s57 offen lds\n\t" \
  "s_waitcnt lgkmcnt(6)\n\t" \
  "v_mfma_f32_32x32x16_bf16 v[176:191], v[220:223], v[92:95], v[176:191]\n\t" \
  "ds_read_b64_tr_b16 v[220:221], v245 offset:6144\n\t" \
  "ds_read_b64_tr_b16 v[222:223], v245 offset:6656\n\t" \
  "v_mfma_f32_32x32x16_bf16 v[176:191], v[224:227], v[96:99], v[176:191]\n\t" \
  "ds_read_b64_tr_b16 v[224:225], v245 offset:8192\n\t" \
  "ds_read_b64_tr_b16 v[226:227], v245 offset:8704\n\t" \
  "s_add_u32 m0, s55, 41344\n\t" \
  "s_add_u32 s57, s53, 43776\n\t" \
  "v_mfma_f32_32x32x16_bf16 v[176:191], v[228:231], v[100:103], v[176:191]\n\t" \
  "ds_read_b64_tr_b16 v[228:229], v245 offset:10240\n\t" \
  "ds_read_b64_tr_b16 v[230:231], v245 offset:10752\n\t" \
  "buffer_load_dwordx4 v250, s[44:47], s57 offen lds\n\t" \
  "s_waitcnt lgkmcnt(6)\n\t" \
  "v_mfma_f32_32x32x16_bf16 v[176:191], v[208:211], v[104:107], v[176:191]\n\t" \
  "ds_read_b64_tr_b16 v[208:209], v245 offset:12288\n\t" \
  "ds_read_b64_tr_b16 v[210:211], v245 offset:12800\n\t" \
  "v_mfma_f32_32x32x16_bf16 v[176:191], v[212:215], v[108:111], v[176:191]\n\t" \
  "ds_read_b64_tr_b16 v[212:213], v245 offset:14336\n\t" \
  "ds_read_b64_tr_b16 v[214:215], v245 offset:14848\n\t" \
  "s_add_u32 m0, s55, 42432\n\t" \
  "s_add_u32 s57, s53, 44928\n\t" \
  "v_mfma_f32_32x32x16_bf16 v[176:191], v[216:219], v[112:115], v[176:191]\n\t" \
  "ds_read_b64_tr_b16 v[216:217], v245 offset:16384\n\t" \
  "ds_read_b64_tr_b16 v[218:219], v245 offset:16896\n\t" \
  "buffer_load_dwordx4 v250, s[44:47], s57 offen lds\n\t" \
  "s_waitcnt lgkmcnt(6)\n\t" \
  "v_mfma_f32_32x32x16_bf16 v[176:191], v[220:223], v[116:119], v[176:191]\n\t" \
  "ds_read_b64_tr_b16 v[220:221], v245 offset:18432\n\t" \
  "ds_read_b64_tr_b16 v[222:223], v245 offset:18944\n\t" \
  "v_mfma_f32_32x32x16_bf16 v[176:191], v[224:227], v[120:123], v[176:191]\n\t" \
  "ds_read_b64_tr_b16 v[224:225], v245 offset:20480\n\t" \
  "ds_read_b64_tr_b16 v[226:227], v245 offset:20992\n\t" \
  "s_add_u32 m0, s56, 4096\n\t" \
  "s_add_u32 s57, s53, 36864\n\t" \
  "v_mfma_f32_32x32x16_bf16 v[176:191], v[228:231], v[124:127], v[176:191]\n\t" \
  "ds_read_b64_tr_b16 v[228:229], v245 offset:22528\n\t" \
  "ds_read_b64_tr_b16 v[230:231], v245 offset:23040\n\t" \
  "buffer_load_dwordx4 v251, s[44:47], s57 offen lds\n\t" \
  "s_cmp_eq_u32 s59, 1\n\t" \
  "s_cselect_b32 s63, s62, 73728\n\t" \
  "s_add_u32 s53, s53, s63\n\t" \
  "s_waitcnt lgkmcnt(6)\n\t" \
  "v_mfma_f32_32x32x16_bf16 v[176:191], v[208:211], v[128:131], v[176:191]\n\t" \
  "ds_read_b64_tr_b16 v[208:209], v245 offset:24576\n\t" \
  "ds_read_b64_tr_b16 v[210:211], v245 offset:25088\n\t" \
  "v_mfma_f32_32x32x16_bf16 v[176:191], v[212:215], v[132:135], v[176:191]\n\t" \
  "ds_read_b64_tr_b16 v[212:213], v245 offset:26624\n\t" \
  "ds_read_b64_tr_b16 v[214:215], v245 offset:27136\n\t" \
  "v_mfma_f32_32x32x16_bf16 v[176:191], v[216:219], v[136:139], v[176:191]\n\t" \
  "ds_read_b64_tr_b16 v[216:217], v245 offset:28672\n\t" \
  "ds_read_b64_tr_b16 v[218:219], v245 offset:29184\n\t" \
  "s_waitcnt lgkmcnt(6)\n\t" \
  "v_mfma_f32_32x32x16_bf16 v[176:191], v[220:223], v[140:143], v[176:191]\n\t" \
  "ds_read_b64_tr_b16 v[220:221], v245 offset:30720\n\t" \
  "ds_read_b64_tr_b16 v[222:223], v245 offset:31232\n\t" \
  "v_mfma_f32_32x32x16_bf16 v[176:191], v[224:227], v[144:147], v[176:191]\n\t" \
  "ds_read_b64_tr_b16 v[224:225], v245 offset:32768\n\t" \
  "ds_read_b64_tr_b16 v[226:227], v245 offset:33280\n\t" \
  "v_mfma_f32_32x32x16_bf16 v[176:191], v[228:231], v[148:151], v[176:191]\n\t" \
  "ds_read_b64_tr_b16 v[228:229], v245 offset:34816\n\t" \
  "ds_read_b64_tr_b16 v[230:231], v245 offset:35328\n\t" \
  "s_waitcnt lgkmcnt(6)\n\t" \
  "v_mfma_f32_32x32x16_bf16 v[176:191], v[208:211], v[152:155], v[176:191]\n\t" \
  "v_xor_b32 v245, 0x9040, v245\n\t" \
  "ds_read_b64_tr_b16 v[208:209], v245 offset:0\n\t" \
  "ds_read_b64_tr_b16 v[210:211], v245 offset:512\n\t" \
  "v_mfma_f32_32x32x16_bf16 v[176:191], v[212:215], v[156:159], v[176:191]\n\t" \
  "ds_read_b64_tr_b16 v[212:213], v245 offset:2048\n\t" \
  "ds_read_b64_tr_b16 v[214:215], v245 offset:2560\n\t" \
  "v_mfma_f32_32x32x16_bf16 v[176:191], v[216:219], v[160:163], v[176:191]\n\t" \
  "ds_read_b64_tr_b16 v[216:217], v245 offset:4096\n\t" \
  "ds_read_b64_tr_b16 v[218:219], v245 offset:4608\n\t" \
  "s_waitcnt lgkmcnt(6)\n\t" \
  "v_mfma_f32_32x32x16_bf16 v[176:191], v[220:223], v[164:167], v[176:191]\n\t" \
  "ds_read_b64_tr_b16 v[220:221], v245 offset:6144\n\t" \
  "ds_read_b64_tr_b16 v[222:223], v245 offset:6656\n\t" \
  "v_mfma_f32_32x32x16_bf16 v[176:191], v[224:227], v[168:171], v[176:191]\n\t" \
  "ds_read_b64_tr_b16 v[224:225], v245 offset:8192\n\t" \
  "ds_read_b64_tr_b16 v[226:227], v245 offset:8704\n\t" \
  "v_mfma_f32_32x32x16_bf16 v[176:191], v[228:231], v[172:175], v[176:191]\n\t" \
  "ds_read_b64_tr_b16 v[228:229], v245 offset:10240\n\t" \
  "ds_read_b64_tr_b16 v[230:231], v245 offset:10752\n\t" \
  "s_waitcnt lgkmcnt(6)\n\t" \
  "v_mfma_f32_32x32x16_bf16 v[192:207], v[208:211], v[32:35], 0\n\t" \
  "ds_read_b64_tr_b16 v[208:209], v245 offset:12288\n\t" \
  "ds_read_b64_tr_b16 v[210:211], v245 offset:12800\n\t" \
  "v_mfma_f32_32x32x16_bf16 v[192:207], v[212:215], v[36:39], v[192:207]\n\t" \
  "ds_read_b64_tr_b16 v[212:213], v245 offset:14336\n\t" \
  "ds_read_b64_tr_b16 v[214:215], v245 offset:14848\n\t" \
  "v_mfma_f32_32x32x16_bf16 v[192:207], v[216:219], v[40:43], v[192:207]\n\t" \
  "ds_read_b64_tr_b16 v[216:217], v245 offset:16384\n\t" \
  "ds_read_b64_tr_b16 v[218:219], v245 offset:16896\n\t" \
  "s_waitcnt lgkmcnt(6)\n\t" \
  "v_mfma_f32_32x32x16_bf16 v[192:207], v[220:223], v[44:47], v[192:207]\n\t" \
  "ds_read_b64_tr_b16 v[220:221], v245 offset:18432\n\t" \
  "ds_read_b64_tr_b16 v[222:223], v245 offset:18944\n\t" \
  "v_mfma_f32_32x32x16_bf16 v[192:207], v[224:227], v[48:51], v[192:207]\n\t" \
  "ds_read_b64_tr_b16 v[224:225], v245 offset:20480\n\t" \
  "ds_read_b64_tr_b16 v[226:227], v245 offset:20992\n\t" \
  "v_mul_f32 v232, v176, v176\n\t" \
  "v_mul_f32 v233, v177, v177\n\t" \
  "v_mul_f32 v234, v178, v178\n\t" \
  "v_mfma_f32_32x32x16_bf16 v[192:207], v[228:231], v[52:55], v[192:207]\n\t" \
  "ds_read_b64_tr_b16 v[228:229], v245 offset:22528\n\t" \
  "ds_read_b64_tr_b16 v[230:231], v245 offset:23040\n\t" \
  "v_mul_f32 v235, v179, v179\n\t" \
  "v_fma_f32 v232, v232, s58, v244\n\t" \
  "v_fma_f32 v233, v233, s58, v244\n\t" \
  "s_waitcnt lgkmcnt(6)\n\t" \
  "v_mfma_f32_32x32x16_bf16 v[192:207], v[208:211], v[56:59], v[192:207]\n\t" \
  "ds_read_b64_tr_b16 v[208:209], v245 offset:24576\n\t" \
  "ds_read_b64_tr_b16 v[210:211], v245 offset:25088\n\t" \
  "v_fma_f32 v234, v234, s58, v244\n\t" \
  "v_fma_f32 v235, v235, s58, v244\n\t" \
  "v_mul_f32 v232, v176, v232\n\t" \
  "v_mfma_f32_32x32x16_bf16 v[192:207], v[212:215], v[60:63], v[192:207]\n\t" \
  "ds_read_b64_tr_b16 v[212:213], v245 offset:26624\n\t" \
  "ds_read_b64_tr_b16 v[214:215], v245 offset:27136\n\t" \
  "v_mul_f32 v233, v177, v233\n\t" \
  "v_mul_f32 v234, v178, v234\n\t" \
  "v_mul_f32 v235, v179, v235\n\t" \
  "v_mfma_f32_32x32x16_bf16 v[192:207], v[216:219], v[64:67], v[192:207]\n\t" \
  "ds_read_b64_tr_b16 v[216:217], v245 offset:28672\n\t" \
  "ds_read_b64_tr_b16 v[218:219], v245 offset:29184\n\t" \
  "v_exp_f32 v232, v232\n\t" \
  "v_exp_f32 v233, v233\n\t" \
  "v_exp_f32 v234, v234\n\t" \
  "s_waitcnt lgkmcnt(6)\n\t" \
  "v_mfma_f32_32x32x16_bf16 v[192:207], v[220:223], v[68:71], v[192:207]\n\t" \
  "ds_read_b64_tr_b16 v[220:221], v245 offset:30720\n\t" \
  "ds_read_b64_tr_b16 v[222:223], v245 offset:31232\n\t" \
  "v_exp_f32 v235, v235\n\t" \
  "v_add_f32 v232, 1.0, v232\n\t" \
  "v_add_f32 v233, 1.0, v233\n\t" \
  "v_mfma_f32_32x32x16_bf16 v[192:207], v[224:227], v[72:75], v[192:207]\n\t" \
  "ds_read_b64_tr_b16 v[224:225], v245 offset:32768\n\t" \
  "ds_read_b64_tr_b16 v[226:227], v245 offset:33280\n\t" \
  "v_add_f32 v234, 1.0, v234\n\t" \
  "v_add_f32 v235, 1.0, v235\n\t" \
  "v_rcp_f32 v232, v232\n\t" \
  "v_mfma_f32_32x32x16_bf16 v[192:207], v[228:231], v[76:79], v[192:207]\n\t" \
  "ds_read_b64_tr_b16 v[228:229], v245 offset:34816\n\t" \
  "ds_read_b64_tr_b16 v[230:231], v245 offset:35328\n\t" \
  "v_rcp_f32 v233, v233\n\t" \
  "v_rcp_f32 v234, v234\n\t" \
  "v_rcp_f32 v235, v235\n\t" \
  "s_waitcnt lgkmcnt(6)\n\t" \
  "v_mfma_f32_32x32x16_bf16 v[192:207], v[208:211], v[80:83], v[192:207]\n\t" \
  "v_xor_b32 v245, 0x9000, v245\n\t" \
  "ds_read_b64_tr_b16 v[208:209], v245 offset:0\n\t" \
  "ds_read_b64_tr_b16 v[210:211], v245 offset:512\n\t" \
  "v_mul_f32 v232, v176, v232\n\t" \
  "v_mul_f32 v233, v177, v233\n\t" \
  "v_mul_f32 v234, v178, v234\n\t" \
  "v_mfma_f32_32x32x16_bf16 v[192:207], v[212:215], v[84:87], v[192:207]\n\t" \
  "ds_read_b64_tr_b16 v[212:213], v245 offset:2048\n\t" \
  "ds_read_b64_tr_b16 v[214:215], v245 offset:2560\n\t" \
  "v_mul_f32 v235, v179, v235\n\t" \
  "v_cvt_pk_bf16_f32 v176, v232, v233\n\t" \
  "v_cvt_pk_bf16_f32 v177, v234, v235\n\t" \
  "v_mfma_f32_32x32x16_bf16 v[192:207], v[216:219], v[88:91], v[192:207]\n\t" \
  "ds_read_b64_tr_b16 v[216:217], v245 offset:4096\n\t" \
  "ds_read_b64_tr_b16 v[218:219], v245 offset:4608\n\t" \
  "v_mul_f32 v236, v180, v180\n\t" \
  "v_mul_f32 v237, v181, v181\n\t" \
  "v_mul_f32 v238, v182, v182\n\t" \
  "s_waitcnt lgkmcnt(6)\n\t" \
  "v_mfma_f32_32x32x16_bf16 v[192:207], v[220:223], v[92:95], v[192:207]\n\t" \
  "ds_read_b64_tr_b16 v[220:221], v245 offset:6144\n\t" \
  "ds_read_b64_tr_b16 v[222:223], v245 offset:6656\n\t" \
  "v_mul_f32 v239, v183, v183\n\t" \
  "v_fma_f32 v236, v236, s58, v244\n\t" \
  "v_fma_f32 v237, v237, s58, v244\n\t" \
  "s_waitcnt vmcnt(9)\n\t" \
  "s_barrier\n\t" \
  "v_mfma_f32_32x32x16_bf16 v[192:207], v[224:227], v[96:99], v[192:207]\n\t" \
  "ds_read_b64_tr_b16 v[224:225], v245 offset:8192\n\t" \
  "ds_read_b64_tr_b16 v[226:227], v245 offset:8704\n\t" \
  "s_add_u32 m0, s54, 0\n\t" \
  "s_add_u32 s57, s52, 0\n\t" \
  "v_fma_f32 v238, v238, s58, v244\n\t" \
  "v_fma_f32 v239, v239, s58, v244\n\t" \
  "v_mul_f32 v236, v180, v236\n\t" \
  "v_mfma_f32_32x32x16_bf16 v[192:207], v[228:231], v[100:103], v[192:207]\n\t" \
  "ds_read_b64_tr_b16 v[228:229], v245 offset:10240\n\t" \
  "ds_read_b64_tr_b16 v[230:231], v245 offset:10752\n\t" \
  "buffer_load_dwordx4 v249, s[40:43], s57 offen lds\n\t" \
  "v_mul_f32 v237, v181, v237\n\t" \
  "v_mul_f32 v238, v182, v238\n\t" \
  "s_waitcnt lgkmcnt(6)\n\t" \
  "v_mfma_f32_32x32x16_bf16 v[192:207], v[208:211], v[104:107], v[192:207]\n\t" \
  "ds_read_b64_tr_b16 v[208:209], v245 offset:12288\n\t" \
  "ds_read_b64_tr_b16 v[210:211], v245 offset:12800\n\t" \
  "v_mul_f32 v239, v183, v239\n\t" \
  "v_exp_f32 v236, v236\n\t" \
  "v_exp_f32 v237, v237\n\t" \
  "v_mfma_f32_32x32x16_bf16 v[192:207], v[212:215], v[108:111], v[192:207]\n\t" \
  "ds_read_b64_tr_b16 v[212:213], v245 offset:14336\n\t" \
  "ds_read_b64_tr_b16 v[214:215], v245 offset:14848\n\t" \
  "s_add_u32 m0, s54, 1024\n\t" \
  "s_add_u32 s57, s52, 73728\n\t" \
  "v_exp_f32 v238, v238\n\t" \
  "v_exp_f32 v239, v239\n\t" \
  "v_add_f32 v236, 1.0, v236\n\t" \
  "v_mfma_f32_32x32x16_bf16 v[192:207], v[216:219], v[112:115], v[192:207]\n\t" \
  "ds_read_b64_tr_b16 v[216:217], v245 offset:16384\n\t" \
  "ds_read_b64_tr_b16 v[218:219], v245 offset:16896\n\t" \
  "buffer_load_dwordx4 v249, s[40:43], s57 offen lds\n\t" \
  "v_add_f32 v237, 1.0, v237\n\t" \
  "v_add_f32 v238, 1.0, v238\n\t" \
  "v_add_f32 v239, 1.0, v239\n\t" \
  "s_waitcnt lgkmcnt(6)\n\t" \
  "v_mfma_f32_32x32x16_bf16 v[192:207], v[220:223], v[116:119], v[192:207]\n\t" \
  "ds_read_b64_tr_b16 v[220:221], v245 offset:18432\n\t" \
  "ds_read_b64_tr_b16 v[222:223], v245 offset:18944\n\t" \
  "v_rcp_f32 v236, v236\n\t" \
  "v_rcp_f32 v237, v237\n\t" \
  "v_rcp_f32 v238, v238\n\t" \
  "v_mfma_f32_32x32x16_bf16 v[192:207], v[224:227], v[120:123], v[192:207]\n\t" \
  "ds_read_b64_tr_b16 v[224:225], v245 offset:20480\n\t" \
  "ds_read_b64_tr_b16 v[226:227], v245 offset:20992\n\t" \
  "s_add_u32 m0, s54, 2048\n\t" \
  "s_add_u32 s57, s52, 147456\n\t" \
  "v_rcp_f32 v239, v239\n\t" \
  "v_mul_f32 v236, v180, v236\n\t" \
  "v_mul_f32 v237, v181, v237\n\t" \
  "v_mfma_f32_32x32x16_bf16 v[192:207], v[228:231], v[124:127], v[192:207]\n\t" \
  "ds_read_b64_tr_b16 v[228:229], v245 offset:22528\n\t" \
  "ds_read_b64_tr_b16 v[230:231], v245 offset:23040\n\t" \
  "buffer_load_dwordx4 v249, s[40:43], s57 offen lds\n\t" \
  "v_mul_f32 v238, v182, v238\n\t" \
  "v_mul_f32 v239, v183, v239\n\t" \
  "v_cvt_pk_bf16_f32 v178, v236, v237\n\t" \
  "s_waitcnt lgkmcnt(6)\n\t" \
  "v_mfma_f32_32x32x16_bf16 v[192:207], v[208:211], v[128:131], v[192:207]\n\t" \
  "ds_read_b64_tr_b16 v[208:209], v245 offset:24576\n\t" \
  "ds_read_b64_tr_b16 v[210:211], v245 offset:25088\n\t" \
  "v_cvt_pk_bf16_f32 v179, v238, v239\n\t" \
  "v_mul_f32 v232, v184, v184\n\t" \
  "v_mul_f32 v233, v185, v185\n\t" \
  "v_mfma_f32_32x32x16_bf16 v[192:207], v[212:215], v[132:135], v[192:207]\n\t" \
  "ds_read_b64_tr_b16 v[212:213], v245 offset:26624\n\t" \
  "ds_read_b64_tr_b16 v[214:215], v245 offset:27136\n\t" \
  "s_add_u32 m0, s54, 3072\n\t" \
  "s_add_u32 s57, s52, 221184\n\t" \
  "v_mul_f32 v234, v186, v186\n\t" \
  "v_mul_f32 v235, v187, v187\n\t" \
  "v_fma_f32 v232, v232, s58, v244\n\t" \
  "v_mfma_f32_32x32x16_bf16 v[192:207], v[216:219], v[136:139], v[192:207]\n\t" \
  "ds_read_b64_tr_b16 v[216:217], v245 offset:28672\n\t" \
  "ds_read_b64_tr_b16 v[218:219], v245 offset:29184\n\t" \
  "buffer_load_dwordx4 v249, s[40:43], s57 offen lds\n\t" \
  "v_fma_f32 v233, v233, s58, v244\n\t" \
  "v_fma_f32 v234, v234, s58, v244\n\t" \
  "v_fma_f32 v235, v235, s58, v244\n\t" \
  "s_waitcnt lgkmcnt(6)\n\t" \
  "v_mfma_f32_32x32x16_bf16 v[192:207], v[220:223], v[140:143], v[192:207]\n\t" \
  "ds_read_b64_tr_b16 v[220:221], v245 offset:30720\n\t" \
  "ds_read_b64_tr_b16 v[222:223], v245 offset:31232\n\t" \
  "v_mul_f32 v232, v184, v232\n\t" \
  "v_mul_f32 v233, v185, v233\n\t" \
  "v_mul_f32 v234, v186, v234\n\t" \
  "v_mfma_f32_32x32x16_bf16 v[192:207], v[224:227], v[144:147], v[192:207]\n\t" \
  "ds_read_b64_tr_b16 v[224:225], v245 offset:32768\n\t" \
  "ds_read_b64_tr_b16 v[226:227], v245 offset:33280\n\t" \
  "s_add_u32 m0, s54, 4096\n\t" \
  "s_add_u32 s57, s52, 294912\n\t" \
  "v_mul_f32 v235, v187, v235\n\t" \
  "v_exp_f32 v232, v232\n\t" \
  "v_exp_f32 v233, v233\n\t" \
  "v_mfma_f32_32x32x16_bf16 v[192:207], v[228:231], v[148:151], v[192:207]\n\t" \
  "ds_read_b64_tr_b16 v[228:229], v245 offset:34816\n\t" \
  "ds_read_b64_tr_b16 v[230:231], v245 offset:35328\n\t" \
  "buffer_load_dwordx4 v249, s[40:43], s57 offen lds\n\t" \
  "v_exp_f32 v234, v234\n\t" \
  "v_exp_f32 v235, v235\n\t" \
  "v_add_f32 v232, 1.0, v232\n\t" \
  "s_waitcnt lgkmcnt(6)\n\t" \
  "v_mfma_f32_32x32x16_bf16 v[192:207], v[208:211], v[152:155], v[192:207]\n\t" \
  "v_xor_b32 v245, 0x9040, v245\n\t" \
  "ds_read_b64_tr_b16 v[208:209], v246 offset:0\n\t" \
  "ds_read_b64_tr_b16 v[210:211], v246 offset:8704\n\t" \
  "v_add_f32 v233, 1.0, v233\n\t" \
  "v_add_f32 v234, 1.0, v234\n\t" \
  "v_add_f32 v235, 1.0, v235\n\t" \
  "v_mfma_f32_32x32x16_bf16 v[192:207], v[212:215], v[156:159], v[192:207]\n\t" \
  "ds_read_b64_tr_b16 v[212:213], v246 offset:64\n\t" \
  "ds_read_b64_tr_b16 v[214:215], v246 offset:8768\n\t" \
  "s_add_u32 m0, s54, 5120\n\t" \
  "s_add_u32 s57, s52, 368640\n\t" \
  "v_rcp_f32 v232, v232\n\t" \
  "v_rcp_f32 v233, v233\n\t" \
  "v_mfma_f32_32x32x16_bf16 v[192:207], v[216:219], v[160:163], v[192:207]\n\t" \
  "ds_read_b64_tr_b16 v[216:217], v246 offset:128\n\t" \
  "ds_read_b64_tr_b16 v[218:219], v246 offset:8832\n\t" \
  "buffer_load_dwordx4 v249, s[40:43], s57 offen lds\n\t" \
  "v_rcp_f32 v234, v234\n\t" \
  "v_rcp_f32 v235, v235\n\t" \
  "v_mul_f32 v232, v184, v232\n\t" \
  "s_waitcnt lgkmcnt(6)\n\t" \
  "v_mfma_f32_32x32x16_bf16 v[192:207], v[220:223], v[164:167], v[192:207]\n\t" \
  "ds_read_b64_tr_b16 v[220:221], v246 offset:192\n\t" \
  "ds_read_b64_tr_b16 v[222:223], v246 offset:8896\n\t" \
  "v_mul_f32 v233, v185, v233\n\t" \
  "v_mul_f32 v234, v186, v234\n\t" \
  "v_mul_f32 v235, v187, v235\n\t" \
  "s_waitcnt vmcnt(6)\n\t" \
  "s_barrier\n\t" \
  "v_mfma_f32_32x32x16_bf16 v[192:207], v[224:227], v[168:171], v[192:207]\n\t" \
  "ds_read_b64_tr_b16 v[224:225], v246 offset:256\n\t" \
  "ds_read_b64_tr_b16 v[226:227], v246 offset:8960\n\t" \
  "s_add_u32 m0, s54, 6144\n\t" \
  "s_add_u32 s57, s52, 442368\n\t" \
  "v_cvt_pk_bf16_f32 v180, v232, v233\n\t" \
  "v_cvt_pk_bf16_f32 v181, v234, v235\n\t" \
  "v_mul_f32 v236, v188, v188\n\t" \
  "v_mfma_f32_32x32x16_bf16 v[192:207], v[228:231], v[172:175], v[192:207]\n\t" \
  "ds_read_b64_tr_b16 v[228:229], v246 offset:320\n\t" \
  "ds_read_b64_tr_b16 v[230:231], v246 offset:9024\n\t" \
  "buffer_load_dwordx4 v249, s[40:43], s57 offen lds\n\t" \
  "v_mul_f32 v237, v189, v189\n\t" \
  "v_mul_f32 v238, v190, v190\n\t" \
  "v_mul_f32 v239, v191, v191\n\t" \
  "s_waitcnt lgkmcnt(6)\n\t" \
  "v_mfma_f32_32x32x16_bf16 a[0:15], v[208:211], v[176:179], a[0:15]\n\t" \
  "ds_read_b64_tr_b16 v[208:209], v246 offset:384\n\t" \
  "ds_read_b64_tr_b16 v[210:211], v246 offset:9088\n\t" \
  "v_fma_f32 v236, v236, s58, v244\n\t" \
  "v_fma_f32 v237, v237, s58, v244\n\t" \
  "v_fma_f32 v238, v238, s58, v244\n\t" \
  "v_mfma_f32_32x32x16_bf16 a[16:31], v[212:215], v[176:179], a[16:31]\n\t" \
  "ds_read_b64_tr_b16 v[212:213], v246 offset:448\n\t" \
  "ds_read_b64_tr_b16 v[214:215], v246 offset:9152\n\t" \
  "s_add_u32 m0, s54, 7168\n\t" \
  "s_add_u32 s57, s52, 516096\n\t" \
  "v_fma_f32 v239, v239, s58, v244\n\t" \
  "v_mul_f32 v236, v188, v236\n\t" \
  "v_mul_f32 v237, v189, v237\n\t" \
  "v_mfma_f32_32x32x16_bf16 a[32:47], v[216:219], v[176:179], a[32:47]\n\t" \
  "ds_read_b64_tr_b16 v[216:217], v246 offset:512\n\t" \
  "ds_read_b64_tr_b16 v[218:219], v246 offset:9216\n\t" \
  "buffer_load_dwordx4 v249, s[40:43], s57 offen lds\n\t" \
  "v_mul_f32 v238, v190, v238\n\t" \
  "v_mul_f32 v239, v191, v239\n\t" \
  "v_exp_f32 v236, v236\n\t" \
  "s_waitcnt lgkmcnt(6)\n\t" \
  "v_mfma_f32_32x32x16_bf16 a[48:63], v[220:223], v[176:179], a[48:63]\n\t" \
  "ds_read_b64_tr_b16 v[220:221], v246 offset:576\n\t" \
  "ds_read_b64_tr_b16 v[222:223], v246 offset:9280\n\t" \
  "v_exp_f32 v237, v237\n\t" \
  "v_exp_f32 v238, v238\n\t" \
  "v_exp_f32 v239, v239\n\t" \
  "v_mfma_f32_32x32x16_bf16 a[64:79], v[224:227], v[176:179], a[64:79]\n\t" \
  "ds_read_b64_tr_b16 v[224:225], v246 offset:640\n\t" \
  "ds_read_b64_tr_b16 v[226:227], v246 offset:9344\n\t" \
  "s_add_u32 m0, s54, 8192\n\t" \
  "s_add_u32 s57, s52, 589824\n\t" \
  "v_add_f32 v236, 1.0, v236\n\t" \
  "v_add_f32 v237, 1.0, v237\n\t" \
  "v_add_f32 v238, 1.0, v238\n\t" \
  "v_mfma_f32_32x32x16_bf16 a[80:95], v[228:231], v[176:179], a[80:95]\n\t" \
  "ds_read_b64_tr_b16 v[228:229], v246 offset:704\n\t" \
  "ds_read_b64_tr_b16 v[230:231], v246 offset:9408\n\t" \
  "buffer_load_dwordx4 v249, s[40:43], s57 offen lds\n\t" \
  "v_add_f32 v239, 1.0, v239\n\t" \
  "v_rcp_f32 v236, v236\n\t" \
  "v_rcp_f32 v237, v237\n\t" \
  "s_waitcnt lgkmcnt(6)\n\t" \
  "v_mfma_f32_32x32x16_bf16 a[96:111], v[208:211], v[176:179], a[96:111]\n\t" \
  "ds_read_b64_tr_b16 v[208:209], v246 offset:768\n\t" \
  "ds_read_b64_tr_b16 v[210:211], v246 offset:9472\n\t" \
  "v_rcp_f32 v238, v238\n\t" \
  "v_rcp_f32 v239, v239\n\t" \
  "v_mul_f32 v236, v188, v236\n\t" \
  "v_mfma_f32_32x32x16_bf16 a[112:127], v[212:215], v[176:179], a[112:127]\n\t" \
  "ds_read_b64_tr_b16 v[212:213], v246 offset:832\n\t" \
  "ds_read_b64_tr_b16 v[214:215], v246 offset:9536\n\t" \
  "s_add_u32 m0, s54, 36864\n\t" \
  "s_add_u32 s57, s52, 2654208\n\t" \
  "v_mul_f32 v237, v189, v237\n\t" \
  "v_mul_f32 v238, v190, v238\n\t" \
  "v_mul_f32 v239, v191, v239\n\t" \
  "v_mfma_f32_32x32x16_bf16 a[128:143], v[216:219], v[176:179], a[128:143]\n\t" \
  "ds_read_b64_tr_b16 v[216:217], v246 offset:896\n\t" \
  "ds_read_b64_tr_b16 v[218:219], v246 offset:9600\n\t" \
  "buffer_load_dwordx4 v249, s[40:43], s57 offen lds\n\t" \
  "v_cvt_pk_bf16_f32 v182, v236, v237\n\t" \
  "v_cvt_pk_bf16_f32 v183, v238, v239\n\t" \
  "s_waitcnt lgkmcnt(6)\n\t" \
  "v_mfma_f32_32x32x16_bf16 a[144:159], v[220:223], v[176:179], a[144:159]\n\t" \
  "ds_read_b64_tr_b16 v[220:221], v246 offset:960\n\t" \
  "ds_read_b64_tr_b16 v[222:223], v246 offset:9664\n\t" \
  "v_mul_f32 v232, v192, v192\n\t" \
  "v_mul_f32 v233, v193, v193\n\t" \
  "v_mul_f32 v234, v194, v194\n\t" \
  "v_mfma_f32_32x32x16_bf16 a[160:175], v[224:227], v[176:179], a[160:175]\n\t" \
  "ds_read_b64_tr_b16 v[224:225], v247 offset:0\n\t" \
  "ds_read_b64_tr_b16 v[226:227], v247 offset:1024\n\t" \
  "s_add_u32 m0, s54, 37888\n\t" \
  "s_add_u32 s57, s52, 2727936\n\t" \
  "v_mul_f32 v235, v195, v195\n\t" \
  "v_fma_f32 v232, v232, s58, v244\n\t" \
  "v_fma_f32 v233, v233, s58, v244\n\t" \
  "v_mfma_f32_32x32x16_bf16 a[176:191], v[228:231], v[176:179], a[176:191]\n\t" \
  "ds_read_b64_tr_b16 v[228:229], v248 offset:0\n\t" \
  "ds_read_b64_tr_b16 v[230:231], v248 offset:1024\n\t" \
  "buffer_load_dwordx4 v249, s[40:43], s57 offen lds\n\t" \
  "v_fma_f32 v234, v234, s58, v244\n\t" \
  "v_fma_f32 v235, v235, s58, v244\n\t" \
  "v_mul_f32 v232, v192, v232\n\t" \
  "s_waitcnt lgkmcnt(6)\n\t" \
  "v_mfma_f32_32x32x16_bf16 a[192:207], v[208:211], v[176:179], a[192:207]\n\t" \
  "ds_read_b64_tr_b16 v[208:209], v246 offset:17408\n\t" \
  "ds_read_b64_tr_b16 v[210:211], v246 offset:26112\n\t" \
  "v_mul_f32 v233, v193, v233\n\t" \
  "v_mul_f32 v234, v194, v234\n\t" \
  "v_mul_f32 v235, v195, v235\n\t" \
  "v_mfma_f32_32x32x16_bf16 a[208:223], v[212:215], v[176:179], a[208:223]\n\t" \
  "ds_read_b64_tr_b16 v[212:213], v246 offset:17472\n\t" \
  "ds_read_b64_tr_b16 v[214:215], v246 offset:26176\n\t" \
  "s_add_u32 m0, s54, 38912\n\t" \
  "s_add_u32 s57, s52, 2801664\n\t" \
  "v_exp_f32 v232, v232\n\t" \
  "v_exp_f32 v233, v233\n\t" \
  "v_exp_f32 v234, v234\n\t" \
  "v_mfma_f32_32x32x16_bf16 a[224:239], v[216:219], v[176:179], a[224:239]\n\t" \
  "ds_read_b64_tr_b16 v[216:217], v246 offset:17536\n\t" \
  "ds_read_b64_tr_b16 v[218:219], v246 offset:26240\n\t" \
  "buffer_load_dwordx4 v249, s[40:43], s57 offen lds\n\t" \
  "v_exp_f32 v235, v235\n\t" \
  "v_add_f32 v232, 1.0, v232\n\t" \
  "v_add_f32 v233, 1.0, v233\n\t" \
  "s_waitcnt lgkmcnt(6)\n\t" \
  "v_mfma_f32_32x32x16_bf16 a[240:255], v[220:223], v[176:179], a[240:255]\n\t" \
  "ds_read_b64_tr_b16 v[220:221], v246 offset:17600\n\t" \
  "ds_read_b64_tr_b16 v[222:223], v246 offset:26304\n\t" \
  "v_add_f32 v234, 1.0, v234\n\t" \
  "v_add_f32 v235, 1.0, v235\n\t" \
  "v_rcp_f32 v232, v232\n\t" \
  "v_mfma_f32_32x32x16_bf16 v[0:15], v[224:227], v[176:179], v[0:15]\n\t" \
  "ds_read_b64_tr_b16 v[224:225], v246 offset:17664\n\t" \
  "ds_read_b64_tr_b16 v[226:227], v246 offset:26368\n\t" \
  "s_add_u32 m0, s54, 39936\n\t" \
  "s_add_u32 s57, s52, 2875392\n\t" \
  "v_rcp_f32 v233, v233\n\t" \
  "v_rcp_f32 v234, v234\n\t" \
  "v_rcp_f32 v235, v235\n\t" \
  "v_mfma_f32_32x32x16_bf16 v[16:31], v[228:231], v[176:179], v[16:31]\n\t" \
  "ds_read_b64_tr_b16 v[228:229], v246 offset:17728\n\t" \
  "ds_read_b64_tr_b16 v[230:231], v246 offset:26432\n\t" \
  "buffer_load_dwordx4 v249, s[40:43], s57 offen lds\n\t" \
  "v_mul_f32 v232, v192, v232\n\t" \
  "v_mul_f32 v233, v193, v233\n\t" \
  "v_mul_f32 v234, v194, v234\n\t" \
  "s_waitcnt lgkmcnt(6)\n\t" \
  "v_mfma_f32_32x32x16_bf16 a[0:15], v[208:211], v[180:183], a[0:15]\n\t" \
  "ds_read_b64_tr_b16 v[208:209], v246 offset:17792\n\t" \
  "ds_read_b64_tr_b16 v[210:211], v246 offset:26496\n\t" \
  "v_mul_f32 v235, v195, v235\n\t" \
  "v_cvt_pk_bf16_f32 v192, v232, v233\n\t" \
  "v_cvt_pk_bf16_f32 v193, v234, v235\n\t" \
  "v_mfma_f32_32x32x16_bf16 a[16:31], v[212:215], v[180:183], a[16:31]\n\t" \
  "ds_read_b64_tr_b16 v[212:213], v246 offset:17856\n\t" \
  "ds_read_b64_tr_b16 v[214:215], v246 offset:26560\n\t" \
  "s_add_u32 m0, s54, 40960\n\t" \
  "s_add_u32 s57, s52, 2949120\n\t" \
  "v_mul_f32 v236, v196, v196\n\t" \
  "v_mul_f32 v237, v197, v197\n\t" \
  "v_mul_f32 v238, v198, v198\n\t" \
  "v_mfma_f32_32x32x16_bf16 a[32:47], v[216:219], v[180:183], a[32:47]\n\t" \
  "ds_read_b64_tr_b16 v[216:217], v246 offset:17920\n\t" \
  "ds_read_b64_tr_b16 v[218:219], v246 offset:26624\n\t" \
  "buffer_load_dwordx4 v249, s[40:43], s57 offen lds\n\t" \
  "v_mul_f32 v239, v199, v199\n\t" \
  "v_fma_f32 v236, v236, s58, v244\n\t" \
  "v_fma_f32 v237, v237, s58, v244\n\t" \
  "s_waitcnt lgkmcnt(6)\n\t" \
  "v_mfma_f32_32x32x16_bf16 a[48:63], v[220:223], v[180:183], a[48:63]\n\t" \
  "ds_read_b64_tr_b16 v[220:221], v246 offset:17984\n\t" \
  "ds_read_b64_tr_b16 v[222:223], v246 offset:26688\n\t" \
  "v_fma_f32 v238, v238, s58, v244\n\t" \
  "v_fma_f32 v239, v239, s58, v244\n\t" \
  "v_mul_f32 v236, v196, v236\n\t" \
  "v_mfma_f32_32x32x16_bf16 a[64:79], v[224:227], v[180:183], a[64:79]\n\t" \
  "ds_read_b64_tr_b16 v[224:225], v246 offset:18048\n\t" \
  "ds_read_b64_tr_b16 v[226:227], v246 offset:26752\n\t" \
  "s_add_u32 m0, s54, 41984\n\t" \
  "s_add_u32 s57, s52, 3022848\n\t" \
  "v_mul_f32 v237, v197, v237\n\t" \
  "v_mul_f32 v238, v198, v238\n\t" \
  "v_mfma_f32_32x32x16_bf16 a[80:95], v[228:231], v[180:183], a[80:95]\n\t" \
  "ds_read_b64_tr_b16 v[228:229], v246 offset:18112\n\t" \
  "ds_read_b64_tr_b16 v[230:231], v246 offset:26816\n\t" \
  "buffer_load_dwordx4 v249, s[40:43], s57 offen lds\n\t" \
  "v_mul_f32 v239, v199, v239\n\t" \
  "v_exp_f32 v236, v236\n\t" \
  "v_exp_f32 v237, v237\n\t" \
  "s_waitcnt lgkmcnt(6)\n\t" \
  "v_mfma_f32_32x32x16_bf16 a[96:111], v[208:211], v[180:183], a[96:111]\n\t" \
  "ds_read_b64_tr_b16 v[208:209], v246 offset:18176\n\t" \
  "ds_read_b64_tr_b16 v[210:211], v246 offset:26880\n\t" \
  "v_exp_f32 v238, v238\n\t" \
  "v_exp_f32 v239, v239\n\t" \
  "v_add_f32 v236, 1.0, v236\n\t" \
  "v_mfma_f32_32x32x16_bf16 a[112:127], v[212:215], v[180:183], a[112:127]\n\t" \
  "ds_read_b64_tr_b16 v[212:213], v246 offset:18240\n\t" \
  "ds_read_b64_tr_b16 v[214:215], v246 offset:26944\n\t" \
  "s_add_u32 m0, s54, 43008\n\t" \
  "s_add_u32 s57, s52, 3096576\n\t" \
  "v_add_f32 v237, 1.0, v237\n\t" \
  "v_add_f32 v238, 1.0, v238\n\t" \
  "v_add_f32 v239, 1.0, v239\n\t" \
  "v_mfma_f32_32x32x16_bf16 a[128:143], v[216:219], v[180:183], a[128:143]\n\t" \
  "ds_read_b64_tr_b16 v[216:217], v246 offset:18304\n\t" \
  "ds_read_b64_tr_b16 v[218:219], v246 offset:27008\n\t" \
  "buffer_load_dwordx4 v249, s[40:43], s57 offen lds\n\t" \
  "v_rcp_f32 v236, v236\n\t" \
  "v_rcp_f32 v237, v237\n\t" \
  "v_rcp_f32 v238, v238\n\t" \
  "s_waitcnt lgkmcnt(6)\n\t" \
  "v_mfma_f32_32x32x16_bf16 a[144:159], v[220:223], v[180:183], a[144:159]\n\t" \
  "ds_read_b64_tr_b16 v[220:221], v246 offset:18368\n\t" \
  "ds_read_b64_tr_b16 v[222:223], v246 offset:27072\n\t" \
  "v_rcp_f32 v239, v239\n\t" \
  "v_mul_f32 v236, v196, v236\n\t" \
  "v_mul_f32 v237, v197, v237\n\t" \
  "v_mfma_f32_32x32x16_bf16 a[160:175], v[224:227], v[180:183], a[160:175]\n\t" \
  "ds_read_b64_tr_b16 v[224:225], v247 offset:2048\n\t" \
  "ds_read_b64_tr_b16 v[226:227], v247 offset:3072\n\t" \
  "s_add_u32 m0, s54, 44032\n\t" \
  "s_add_u32 s57, s52, 3170304\n\t" \
  "v_mul_f32 v238, v198, v238\n\t" \
  "v_mul_f32 v239, v199, v239\n\t" \
  "v_cvt_pk_bf16_f32 v194, v236, v237\n\t" \
  "v_mfma_f32_32x32x16_bf16 a[176:191], v[228:231], v[180:183], a[176:191]\n\t" \
  "ds_read_b64_tr_b16 v[228:229], v248 offset:2048\n\t" \
  "ds_read_b64_tr_b16 v[230:231], v248 offset:3072\n\t" \
  "buffer_load_dwordx4 v249, s[40:43], s57 offen lds\n\t" \
  "v_cvt_pk_bf16_f32 v195, v238, v239\n\t" \
  "v_mul_f32 v232, v200, v200\n\t" \
  "v_mul_f32 v233, v201, v201\n\t" \
  "s_waitcnt lgkmcnt(6)\n\t" \
  "v_mfma_f32_32x32x16_bf16 a[192:207], v[208:211], v[180:183], a[192:207]\n\t" \
  "ds_read_b64_tr_b16 v[208:209], v246 offset:34816\n\t" \
  "ds_read_b64_tr_b16 v[210:211], v246 offset:43520\n\t" \
  "v_mul_f32 v234, v202, v202\n\t" \
  "v_mul_f32 v235, v203, v203\n\t" \
  "v_fma_f32 v232, v232, s58, v244\n\t" \
  "v_mfma_f32_32x32x16_bf16 a[208:223], v[212:215], v[180:183], a[208:223]\n\t" \
  "ds_read_b64_tr_b16 v[212:213], v246 offset:34880\n\t" \
  "ds_read_b64_tr_b16 v[214:215], v246 offset:43584\n\t" \
  "s_add_u32 m0, s54, 45056\n\t" \
  "s_add_u32 s57, s52, 3244032\n\t" \
  "v_fma_f32 v233, v233, s58, v244\n\t" \
  "v_fma_f32 v234, v234, s58, v244\n\t" \
  "v_fma_f32 v235, v235, s58, v244\n\t" \
  "v_mfma_f32_32x32x16_bf16 a[224:239], v[216:219], v[180:183], a[224:239]\n\t" \
  "ds_read_b64_tr_b16 v[216:217], v246 offset:34944\n\t" \
  "ds_read_b64_tr_b16 v[218:219], v246 offset:43648\n\t" \
  "buffer_load_dwordx4 v249, s[40:43], s57 offen lds\n\t" \
  "s_cmp_eq_u32 s59, 2\n\t" \
  "s_cselect_b32 s63, s61, 128\n\t" \
  "s_add_u32 s52, s52, s63\n\t" \
  "v_mul_f32 v232, v200, v232\n\t" \
  "v_mul_f32 v233, v201, v233\n\t" \
  "v_mul_f32 v234, v202, v234\n\t" \
  "s_waitcnt lgkmcnt(6)\n\t" \
  "v_mfma_f32_32x32x16_bf16 a[240:255], v[220:223], v[180:183], a[240:255]\n\t" \
  "ds_read_b64_tr_b16 v[220:221], v246 offset:35008\n\t" \
  "ds_read_b64_tr_b16 v[222:223], v246 offset:43712\n\t" \
  "v_mul_f32 v235, v203, v235\n\t" \
  "v_exp_f32 v232, v232\n\t" \
  "v_exp_f32 v233, v233\n\t" \
  "s_waitcnt vmcnt(9)\n\t" \
  "s_barrier\n\t" \
  "v_mfma_f32_32x32x16_bf16 v[0:15], v[224:227], v[180:183], v[0:15]\n\t" \
  "ds_read_b64_tr_b16 v[224:225], v246 offset:35072\n\t" \
  "ds_read_b64_tr_b16 v[226:227], v246 offset:43776\n\t" \
  "s_add_u32 m0, s55, 0\n\t" \
  "s_add_u32 s57, s53, 0\n\t" \
  "v_exp_f32 v234, v234\n\t" \
  "v_exp_f32 v235, v235\n\t" \
  "v_add_f32 v232, 1.0, v232\n\t" \
  "v_mfma_f32_32x32x16_bf16 v[16:31], v[228:231], v[180:183], v[16:31]\n\t" \
  "ds_read_b64_tr_b16 v[228:229], v246 offset:35136\n\t" \
  "ds_read_b64_tr_b16 v[230:231], v246 offset:43840\n\t" \
  "buffer_load_dwordx4 v250, s[44:47], s57 offen lds\n\t" \
  "v_add_f32 v233, 1.0, v233\n\t" \
  "v_add_f32 v234, 1.0, v234\n\t" \
  "v_add_f32 v235, 1.0, v235\n\t" \
  "s_cmp_eq_u32 s59, 1\n\t" \
  "s_cbranch_scc1 MLP_LAST_%=\n\t" \
  "s_waitcnt lgkmcnt(6)\n\t" \
  "v_mfma_f32_32x32x16_bf16 a[0:15], v[208:211], v[192:195], a[0:15]\n\t" \
  "ds_read_b64_tr_b16 v[208:209], v246 offset:35200\n\t" \
  "ds_read_b64_tr_b16 v[210:211], v246 offset:43904\n\t" \
  "v_rcp_f32 v232, v232\n\t" \
  "v_rcp_f32 v233, v233\n\t" \
  "v_mfma_f32_32x32x16_bf16 a[16:31], v[212:215], v[192:195], a[16:31]\n\t" \
  "ds_read_b64_tr_b16 v[212:213], v246 offset:35264\n\t" \
  "ds_read_b64_tr_b16 v[214:215], v246 offset:43968\n\t" \
  "s_add_u32 m0, s55, 1088\n\t" \
  "s_add_u32 s57, s53, 1152\n\t" \
  "v_rcp_f32 v234, v234\n\t" \
  "v_rcp_f32 v235, v235\n\t" \
  "v_mul_f32 v232, v200, v232\n\t" \
  "v_mfma_f32_32x32x16_bf16 a[32:47], v[216:219], v[192:195], a[32:47]\n\t" \
  "ds_read_b64_tr_b16 v[216:217], v246 offset:35328\n\t" \
  "ds_read_b64_tr_b16 v[218:219], v246 offset:44032\n\t" \
  "buffer_load_dwordx4 v250, s[44:47], s57 offen lds\n\t" \
  "v_mul_f32 v233, v201, v233\n\t" \
  "v_mul_f32 v234, v202, v234\n\t" \
  "v_mul_f32 v235, v203, v235\n\t" \
  "s_waitcnt lgkmcnt(6)\n\t" \
  "v_mfma_f32_32x32x16_bf16 a[48:63], v[220:223], v[192:195], a[48:63]\n\t" \
  "ds_read_b64_tr_b16 v[220:221], v246 offset:35392\n\t" \
  "ds_read_b64_tr_b16 v[222:223], v246 offset:44096\n\t" \
  "v_cvt_pk_bf16_f32 v196, v232, v233\n\t" \
  "v_cvt_pk_bf16_f32 v197, v234, v235\n\t" \
  "v_mul_f32 v236, v204, v204\n\t" \
  "v_mfma_f32_32x32x16_bf16 a[64:79], v[224:227], v[192:195], a[64:79]\n\t" \
  "ds_read_b64_tr_b16 v[224:225], v246 offset:35456\n\t" \
  "ds_read_b64_tr_b16 v[226:227], v246 offset:44160\n\t" \
  "s_add_u32 m0, s55, 2176\n\t" \
  "s_add_u32 s57, s53, 2304\n\t" \
  "v_mul_f32 v237, v205, v205\n\t" \
  "v_mul_f32 v238, v206, v206\n\t" \
  "v_mul_f32 v239, v207, v207\n\t" \
  "v_mfma_f32_32x32x16_bf16 a[80:95], v[228:231], v[192:195], a[80:95]\n\t" \
  "ds_read_b64_tr_b16 v[228:229], v246 offset:35520\n\t" \
  "ds_read_b64_tr_b16 v[230:231], v246 offset:44224\n\t" \
  "buffer_load_dwordx4 v250, s[44:47], s57 offen lds\n\t" \
  "v_fma_f32 v236, v236, s58, v244\n\t" \
  "v_fma_f32 v237, v237, s58, v244\n\t" \
  "v_fma_f32 v238, v238, s58, v244\n\t" \
  "s_waitcnt lgkmcnt(6)\n\t" \
  "v_mfma_f32_32x32x16_bf16 a[96:111], v[208:211], v[192:195], a[96:111]\n\t" \
  "ds_read_b64_tr_b16 v[208:209], v246 offset:35584\n\t" \
  "ds_read_b64_tr_b16 v[210:211], v246 offset:44288\n\t" \
  "v_fma_f32 v239, v239, s58, v244\n\t" \
  "v_mul_f32 v236, v204, v236\n\t" \
  "v_mul_f32 v237, v205, v237\n\t" \
  "v_mfma_f32_32x32x16_bf16 a[112:127], v[212:215], v[192:195], a[112:127]\n\t" \
  "ds_read_b64_tr_b16 v[212:213], v246 offset:35648\n\t" \
  "ds_read_b64_tr_b16 v[214:215], v246 offset:44352\n\t" \
  "s_add_u32 m0, s55, 3264\n\t" \
  "s_add_u32 s57, s53, 3456\n\t" \
  "v_mul_f32 v238, v206, v238\n\t" \
  "v_mul_f32 v239, v207, v239\n\t" \
  "v_exp_f32 v236, v236\n\t" \
  "v_mfma_f32_32x32x16_bf16 a[128:143], v[216:219], v[192:195], a[128:143]\n\t" \
  "ds_read_b64_tr_b16 v[216:217], v246 offset:35712\n\t" \
  "ds_read_b64_tr_b16 v[218:219], v246 offset:44416\n\t" \
  "buffer_load_dwordx4 v250, s[44:47], s57 offen lds\n\t" \
  "v_exp_f32 v237, v237\n\t" \
  "v_exp_f32 v238, v238\n\t" \
  "v_exp_f32 v239, v239\n\t" \
  "s_waitcnt lgkmcnt(6)\n\t" \
  "v_mfma_f32_32x32x16_bf16 a[144:159], v[220:223], v[192:195], a[144:159]\n\t" \
  "ds_read_b64_tr_b16 v[220:221], v246 offset:35776\n\t" \
  "ds_read_b64_tr_b16 v[222:223], v246 offset:44480\n\t" \
  "v_add_f32 v236, 1.0, v236\n\t" \
  "v_add_f32 v237, 1.0, v237\n\t" \
  "v_add_f32 v238, 1.0, v238\n\t" \
  "v_mfma_f32_32x32x16_bf16 a[160:175], v[224:227], v[192:195], a[160:175]\n\t" \
  "ds_read_b64_tr_b16 v[224:225], v247 offset:4096\n\t" \
  "ds_read_b64_tr_b16 v[226:227], v247 offset:5120\n\t" \
  "s_add_u32 m0, s55, 4352\n\t" \
  "s_add_u32 s57, s53, 4608\n\t" \
  "v_add_f32 v239, 1.0, v239\n\t" \
  "v_rcp_f32 v236, v236\n\t" \
  "v_rcp_f32 v237, v237\n\t" \
  "v_mfma_f32_32x32x16_bf16 a[176:191], v[228:231], v[192:195], a[176:191]\n\t" \
  "ds_read_b64_tr_b16 v[228:229], v248 offset:4096\n\t" \
  "ds_read_b64_tr_b16 v[230:231], v248 offset:5120\n\t" \
  "buffer_load_dwordx4 v250, s[44:47], s57 offen lds\n\t" \
  "v_rcp_f32 v238, v238\n\t" \
  "v_rcp_f32 v239, v239\n\t" \
  "v_mul_f32 v236, v204, v236\n\t" \
  "s_waitcnt lgkmcnt(6)\n\t" \
  "v_mfma_f32_32x32x16_bf16 a[192:207], v[208:211], v[192:195], a[192:207]\n\t" \
  "ds_read_b64_tr_b16 v[208:209], v246 offset:52224\n\t" \
  "ds_read_b64_tr_b16 v[210:211], v246 offset:60928\n\t" \
  "v_mul_f32 v237, v205, v237\n\t" \
  "v_mul_f32 v238, v206, v238\n\t" \
  "v_mul_f32 v239, v207, v239\n\t" \
  "v_mfma_f32_32x32x16_bf16 a[208:223], v[212:215], v[192:195], a[208:223]\n\t" \
  "ds_read_b64_tr_b16 v[212:213], v246 offset:52288\n\t" \
  "ds_read_b64_tr_b16 v[214:215], v246 offset:60992\n\t" \
  "s_add_u32 m0, s55, 5440\n\t" \
  "s_add_u32 s57, s53, 5760\n\t" \
  "v_cvt_pk_bf16_f32 v198, v236, v237\n\t" \
  "v_cvt_pk_bf16_f32 v199, v238, v239\n\t" \
  "v_mfma_f32_32x32x16_bf16 a[224:239], v[216:219], v[192:195], a[224:239]\n\t" \
  "ds_read_b64_tr_b16 v[216:217], v246 offset:52352\n\t" \
  "ds_read_b64_tr_b16 v[218:219], v246 offset:61056\n\t" \
  "buffer_load_dwordx4 v250, s[44:47], s57 offen lds\n\t" \
  "s_waitcnt lgkmcnt(6)\n\t" \
  "v_mfma_f32_32x32x16_bf16 a[240:255], v[220:223], v[192:195], a[240:255]\n\t" \
  "ds_read_b64_tr_b16 v[220:221], v246 offset:52416\n\t" \
  "ds_read_b64_tr_b16 v[222:223], v246 offset:61120\n\t" \
  "v_mfma_f32_32x32x16_bf16 v[0:15], v[224:227], v[192:195], v[0:15]\n\t" \
  "ds_read_b64_tr_b16 v[224:225], v246 offset:52480\n\t" \
  "ds_read_b64_tr_b16 v[226:227], v246 offset:61184\n\t" \
  "s_add_u32 m0, s55, 6528\n\t" \
  "s_add_u32 s57, s53, 6912\n\t" \
  "v_mfma_f32_32x32x16_bf16 v[16:31], v[228:231], v[192:195], v[16:31]\n\t" \
  "ds_read_b64_tr_b16 v[228:229], v246 offset:52544\n\t" \
  "ds_read_b64_tr_b16 v[230:231], v246 offset:61248\n\t" \
  "buffer_load_dwordx4 v250, s[44:47], s57 offen lds\n\t" \
  "s_waitcnt lgkmcnt(6)\n\t" \
  "v_mfma_f32_32x32x16_bf16 a[0:15], v[208:211], v[196:199], a[0:15]\n\t" \
  "ds_read_b64_tr_b16 v[208:209], v246 offset:52608\n\t" \
  "ds_read_b64_tr_b16 v[210:211], v246 offset:61312\n\t" \
  "v_mfma_f32_32x32x16_bf16 a[16:31], v[212:215], v[196:199], a[16:31]\n\t" \
  "ds_read_b64_tr_b16 v[212:213], v246 offset:52672\n\t" \
  "ds_read_b64_tr_b16 v[214:215], v246 offset:61376\n\t" \
  "s_add_u32 m0, s55, 7616\n\t" \
  "s_add_u32 s57, s53, 8064\n\t" \
  "v_mfma_f32_32x32x16_bf16 a[32:47], v[216:219], v[196:199], a[32:47]\n\t" \
  "ds_read_b64_tr_b16 v[216:217], v246 offset:52736\n\t" \
  "ds_read_b64_tr_b16 v[218:219], v246 offset:61440\n\t" \
  "buffer_load_dwordx4 v250, s[44:47], s57 offen lds\n\t" \
  "s_waitcnt lgkmcnt(6)\n\t" \
  "v_mfma_f32_32x32x16_bf16 a[48:63], v[220:223], v[196:199], a[48:63]\n\t" \
  "ds_read_b64_tr_b16 v[220:221], v246 offset:52800\n\t" \
  "ds_read_b64_tr_b16 v[222:223], v246 offset:61504\n\t" \
  "v_mfma_f32_32x32x16_bf16 a[64:79], v[224:227], v[196:199], a[64:79]\n\t" \
  "ds_read_b64_tr_b16 v[224:225], v246 offset:52864\n\t" \
  "ds_read_b64_tr_b16 v[226:227], v246 offset:61568\n\t" \
  "s_add_u32 m0, s56, 0\n\t" \
  "s_add_u32 s57, s53, 0\n\t" \
  "v_mfma_f32_32x32x16_bf16 a[80:95], v[228:231], v[196:199], a[80:95]\n\t" \
  "ds_read_b64_tr_b16 v[228:229], v246 offset:52928\n\t" \
  "ds_read_b64_tr_b16 v[230:231], v246 offset:61632\n\t" \
  "buffer_load_dwordx4 v251, s[44:47], s57 offen lds\n\t" \
  "s_waitcnt lgkmcnt(6)\n\t" \
  "v_mfma_f32_32x32x16_bf16 a[96:111], v[208:211], v[196:199], a[96:111]\n\t" \
  "ds_read_b64_tr_b16 v[208:209], v246 offset:52992\n\t" \
  "ds_read_b64_tr_b16 v[210:211], v246 offset:61696\n\t" \
  "v_mfma_f32_32x32x16_bf16 a[112:127], v[212:215], v[196:199], a[112:127]\n\t" \
  "ds_read_b64_tr_b16 v[212:213], v246 offset:53056\n\t" \
  "ds_read_b64_tr_b16 v[214:215], v246 offset:61760\n\t" \
  "v_mfma_f32_32x32x16_bf16 a[128:143], v[216:219], v[196:199], a[128:143]\n\t" \
  "ds_read_b64_tr_b16 v[216:217], v246 offset:53120\n\t" \
  "ds_read_b64_tr_b16 v[218:219], v246 offset:61824\n\t" \
  "s_waitcnt lgkmcnt(6)\n\t" \
  "v_mfma_f32_32x32x16_bf16 a[144:159], v[220:223], v[196:199], a[144:159]\n\t" \
  "ds_read_b64_tr_b16 v[220:221], v246 offset:53184\n\t" \
  "ds_read_b64_tr_b16 v[222:223], v246 offset:61888\n\t" \
  "v_mfma_f32_32x32x16_bf16 a[160:175], v[224:227], v[196:199], a[160:175]\n\t" \
  "ds_read_b64_tr_b16 v[224:225], v247 offset:6144\n\t" \
  "ds_read_b64_tr_b16 v[226:227], v247 offset:7168\n\t" \
  "v_mfma_f32_32x32x16_bf16 a[176:191], v[228:231], v[196:199], a[176:191]\n\t" \
  "ds_read_b64_tr_b16 v[228:229], v248 offset:6144\n\t" \
  "ds_read_b64_tr_b16 v[230:231], v248 offset:7168\n\t" \
  "s_waitcnt lgkmcnt(6)\n\t" \
  "v_mfma_f32_32x32x16_bf16 a[192:207], v[208:211], v[196:199], a[192:207]\n\t" \
  "ds_read_b64_tr_b16 v[208:209], v245 offset:0\n\t" \
  "ds_read_b64_tr_b16 v[210:211], v245 offset:512\n\t" \
  "v_mfma_f32_32x32x16_bf16 a[208:223], v[212:215], v[196:199], a[208:223]\n\t" \
  "ds_read_b64_tr_b16 v[212:213], v245 offset:2048\n\t" \
  "ds_read_b64_tr_b16 v[214:215], v245 offset:2560\n\t" \
  "v_mfma_f32_32x32x16_bf16 a[224:239], v[216:219], v[196:199], a[224:239]\n\t" \
  "ds_read_b64_tr_b16 v[216:217], v245 offset:4096\n\t" \
  "ds_read_b64_tr_b16 v[218:219], v245 offset:4608\n\t" \
  "s_waitcnt lgkmcnt(6)\n\t" \
  "v_mfma_f32_32x32x16_bf16 a[240:255], v[220:223], v[196:199], a[240:255]\n\t" \
  "ds_read_b64_tr_b16 v[220:221], v245 offset:6144\n\t" \
  "ds_read_b64_tr_b16 v[222:223], v245 offset:6656\n\t" \
  "s_waitcnt vmcnt(9)\n\t" \
  "s_barrier\n\t" \
  "v_mfma_f32_32x32x16_bf16 v[0:15], v[224:227], v[196:199], v[0:15]\n\t" \
  "ds_read_b64_tr_b16 v[224:225], v245 offset:8192\n\t" \
  "ds_read_b64_tr_b16 v[226:227], v245 offset:8704\n\t" \
  "s_add_u32 m0, s55, 34816\n\t" \
  "s_add_u32 s57, s53, 36864\n\t" \
  "v_mfma_f32_32x32x16_bf16 v[16:31], v[228:231], v[196:199], v[16:31]\n\t" \
  "ds_read_b64_tr_b16 v[228:229], v245 offset:10240\n\t" \
  "ds_read_b64_tr_b16 v[230:231], v245 offset:10752\n\t" \
  "buffer_load_dwordx4 v250, s[44:47], s57 offen lds\n\t" \
  "s_sub_u32 s59, s59, 1\n\t" \
  "s_branch MLP_LOOP_%=\n\t" \
  "MLP_LAST_%=:\n\t" \
  "v_bfe_u32 v253, v252, 4, 1\n\t" \
  "v_mul_u32_u24 v253, 1152, v253\n\t" \
  "v_lshrrev_b32 v254, 5, v252\n\t" \
  "v_lshl_add_u32 v253, v254, 4, v253\n\t" \
  "v_and_b32 v254, 15, v252\n\t" \
  "v_add_u32 v253, v253, v254\n\t" \
  "v_lshlrev_b32 v253, 4, v253\n\t" \
  "s_waitcnt lgkmcnt(6)\n\t" \
  "v_mfma_f32_32x32x16_bf16 a[0:15], v[208:211], v[192:195], a[0:15]\n\t" \
  "ds_read_b64_tr_b16 v[208:209], v246 offset:35200\n\t" \
  "ds_read_b64_tr_b16 v[210:211], v246 offset:43904\n\t" \
  "v_rcp_f32 v232, v232\n\t" \
  "v_rcp_f32 v233, v233\n\t" \
  "v_mfma_f32_32x32x16_bf16 a[16:31], v[212:215], v[192:195], a[16:31]\n\t" \
  "ds_read_b64_tr_b16 v[212:213], v246 offset:35264\n\t" \
  "ds_read_b64_tr_b16 v[214:215], v246 offset:43968\n\t" \
  "v_rcp_f32 v234, v234\n\t" \
  "v_rcp_f32 v235, v235\n\t" \
  "v_mul_f32 v232, v200, v232\n\t" \
  "v_mfma_f32_32x32x16_bf16 a[32:47], v[216:219], v[192:195], a[32:47]\n\t" \
  "ds_read_b64_tr_b16 v[216:217], v246 offset:35328\n\t" \
  "ds_read_b64_tr_b16 v[218:219], v246 offset:44032\n\t" \
  "v_mul_f32 v233, v201, v233\n\t" \
  "v_mul_f32 v234, v202, v234\n\t" \
  "v_mul_f32 v235, v203, v235\n\t" \
  "s_waitcnt lgkmcnt(6)\n\t" \
  "v_mfma_f32_32x32x16_bf16 a[48:63], v[220:223], v[192:195], a[48:63]\n\t" \
  "ds_read_b64_tr_b16 v[220:221], v246 offset:35392\n\t" \
  "ds_read_b64_tr_b16 v[222:223], v246 offset:44096\n\t" \
  "v_cvt_pk_bf16_f32 v196, v232, v233\n\t" \
  "v_cvt_pk_bf16_f32 v197, v234, v235\n\t" \
  "v_mul_f32 v236, v204, v204\n\t" \
  "v_mfma_f32_32x32x16_bf16 a[64:79], v[224:227], v[192:195], a[64:79]\n\t" \
  "ds_read_b64_tr_b16 v[224:225], v246 offset:35456\n\t" \
  "ds_read_b64_tr_b16 v[226:227], v246 offset:44160\n\t" \
  "v_mul_f32 v237, v205, v205\n\t" \
  "v_mul_f32 v238, v206, v206\n\t" \
  "v_mul_f32 v239, v207, v207\n\t" \
  "v_mfma_f32_32x32x16_bf16 a[80:95], v[228:231], v[192:195], a[80:95]\n\t" \
  "ds_read_b64_tr_b16 v[228:229], v246 offset:35520\n\t" \
  "ds_read_b64_tr_b16 v[230:231], v246 offset:44224\n\t" \
  "v_fma_f32 v236, v236, s58, v244\n\t" \
  "v_fma_f32 v237, v237, s58, v244\n\t" \
  "v_fma_f32 v238, v238, s58, v244\n\t" \
  "s_waitcnt lgkmcnt(6)\n\t" \
  "v_mfma_f32_32x32x16_bf16 a[96:111], v[208:211], v[192:195], a[96:111]\n\t" \
  "ds_read_b64_tr_b16 v[208:209], v246 offset:35584\n\t" \
  "ds_read_b64_tr_b16 v[210:211], v246 offset:44288\n\t" \
  "v_fma_f32 v239, v239, s58, v244\n\t" \
  "v_mul_f32 v236, v204, v236\n\t" \
  "v_mul_f32 v237, v205, v237\n\t" \
  "v_mfma_f32_32x32x16_bf16 a[112:127], v[212:215], v[192:195], a[112:127]\n\t" \
  "ds_read_b64_tr_b16 v[212:213], v246 offset:35648\n\t" \
  "ds_read_b64_tr_b16 v[214:215], v246 offset:44352\n\t" \
  "v_mul_f32 v238, v206, v238\n\t" \
  "v_mul_f32 v239, v207, v239\n\t" \
  "v_exp_f32 v236, v236\n\t" \
  "v_mfma_f32_32x32x16_bf16 a[128:143], v[216:219], v[192:195], a[128:143]\n\t" \
  "ds_read_b64_tr_b16 v[216:217], v246 offset:35712\n\t" \
  "ds_read_b64_tr_b16 v[218:219], v246 offset:44416\n\t" \
  "v_exp_f32 v237, v237\n\t" \
  "v_exp_f32 v238, v238\n\t" \
  "v_exp_f32 v239, v239\n\t" \
  "s_waitcnt lgkmcnt(6)\n\t" \
  "v_mfma_f32_32x32x16_bf16 a[144:159], v[220:223], v[192:195], a[144:159]\n\t" \
  "ds_read_b64_tr_b16 v[220:221], v246 offset:35776\n\t" \
  "ds_read_b64_tr_b16 v[222:223], v246 offset:44480\n\t" \
  "v_add_f32 v236, 1.0, v236\n\t" \
  "v_add_f32 v237, 1.0, v237\n\t" \
  "v_add_f32 v238, 1.0, v238\n\t" \
  "v_mfma_f32_32x32x16_bf16 a[160:175], v[224:227], v[192:195], a[160:175]\n\t" \
  "ds_read_b64_tr_b16 v[224:225], v247 offset:4096\n\t" \
  "ds_read_b64_tr_b16 v[226:227], v247 offset:5120\n\t" \
  "v_add_f32 v239, 1.0, v239\n\t" \
  "v_rcp_f32 v236, v236\n\t" \
  "v_rcp_f32 v237, v237\n\t" \
  "v_mfma_f32_32x32x16_bf16 a[176:191], v[228:231], v[192:195], a[176:191]\n\t" \
  "ds_read_b64_tr_b16 v[228:229], v248 offset:4096\n\t" \
  "ds_read_b64_tr_b16 v[230:231], v248 offset:5120\n\t" \
  "v_rcp_f32 v238, v238\n\t" \
  "v_rcp_f32 v239, v239\n\t" \
  "v_mul_f32 v236, v204, v236\n\t" \
  "s_waitcnt lgkmcnt(6)\n\t" \
  "v_mfma_f32_32x32x16_bf16 a[192:207], v[208:211], v[192:195], a[192:207]\n\t" \
  "ds_read_b64_tr_b16 v[208:209], v246 offset:52224\n\t" \
  "ds_read_b64_tr_b16 v[210:211], v246 offset:60928\n\t" \
  "v_mul_f32 v237, v205, v237\n\t" \
  "v_mul_f32 v238, v206, v238\n\t" \
  "v_mul_f32 v239, v207, v239\n\t" \
  "v_mfma_f32_32x32x16_bf16 a[208:223], v[212:215], v[192:195], a[208:223]\n\t" \
  "ds_read_b64_tr_b16 v[212:213], v246 offset:52288\n\t" \
  "ds_read_b64_tr_b16 v[214:215], v246 offset:60992\n\t" \
  "v_cvt_pk_bf16_f32 v198, v236, v237\n\t" \
  "v_cvt_pk_bf16_f32 v199, v238, v239\n\t" \
  "v_mfma_f32_32x32x16_bf16 a[224:239], v[216:219], v[192:195], a[224:239]\n\t" \
  "ds_read_b64_tr_b16 v[216:217], v246 offset:52352\n\t" \
  "ds_read_b64_tr_b16 v[218:219], v246 offset:61056\n\t" \
  "s_waitcnt lgkmcnt(6)\n\t" \
  "v_mfma_f32_32x32x16_bf16 a[240:255], v[220:223], v[192:195], a[240:255]\n\t" \
  "ds_read_b64_tr_b16 v[220:221], v246 offset:52416\n\t" \
  "ds_read_b64_tr_b16 v[222:223], v246 offset:61120\n\t" \
  "v_mfma_f32_32x32x16_bf16 v[0:15], v[224:227], v[192:195], v[0:15]\n\t" \
  "ds_read_b64_tr_b16 v[224:225], v246 offset:52480\n\t" \
  "ds_read_b64_tr_b16 v[226:227], v246 offset:61184\n\t" \
  "v_mfma_f32_32x32x16_bf16 v[16:31], v[228:231], v[192:195], v[16:31]\n\t" \
  "ds_read_b64_tr_b16 v[228:229], v246 offset:52544\n\t" \
  "ds_read_b64_tr_b16 v[230:231], v246 offset:61248\n\t" \
  "s_waitcnt lgkmcnt(6)\n\t" \
  "v_mfma_f32_32x32x16_bf16 a[0:15], v[208:211], v[196:199], a[0:15]\n\t" \
  "ds_read_b64_tr_b16 v[208:209], v246 offset:52608\n\t" \
  "ds_read_b64_tr_b16 v[210:211], v246 offset:61312\n\t" \
  "v_mfma_f32_32x32x16_bf16 a[16:31], v[212:215], v[196:199], a[16:31]\n\t" \
  "ds_read_b64_tr_b16 v[212:213], v246 offset:52672\n\t" \
  "ds_read_b64_tr_b16 v[214:215], v246 offset:61376\n\t" \
  "v_mfma_f32_32x32x16_bf16 a[32:47], v[216:219], v[196:199], a[32:47]\n\t" \
  "ds_read_b64_tr_b16 v[216:217], v246 offset:52736\n\t" \
  "ds_read_b64_tr_b16 v[218:219], v246 offset:61440\n\t" \
  "s_waitcnt lgkmcnt(6)\n\t" \
  "v_mfma_f32_32x32x16_bf16 a[48:63], v[220:223], v[196:199], a[48:63]\n\t" \
  "ds_read_b64_tr_b16 v[220:221], v246 offset:52800\n\t" \
  "ds_read_b64_tr_b16 v[222:223], v246 offset:61504\n\t" \
  "v_accvgpr_read_b32 v32, a0\n\t" \
  "v_accvgpr_read_b32 v33, a1\n\t" \
  "v_accvgpr_read_b32 v34, a2\n\t" \
  "v_accvgpr_read_b32 v35, a3\n\t" \
  "v_accvgpr_read_b32 v36, a4\n\t" \
  "v_accvgpr_read_b32 v37, a5\n\t" \
  "v_accvgpr_read_b32 v38, a6\n\t" \
  "v_accvgpr_read_b32 v39, a7\n\t" \
  "v_accvgpr_read_b32 v40, a8\n\t" \
  "v_accvgpr_read_b32 v41, a9\n\t" \
  "v_accvgpr_read_b32 v42, a10\n\t" \
  "v_accvgpr_read_b32 v43, a11\n\t" \
  "v_accvgpr_read_b32 v44, a12\n\t" \
  "v_accvgpr_read_b32 v45, a13\n\t" \
  "v_accvgpr_read_b32 v46, a14\n\t" \
  "v_accvgpr_read_b32 v47, a15\n\t" \
  "v_cvt_pk_bf16_f32 v48, v32, v33\n\t" \
  "v_cvt_pk_bf16_f32 v49, v34, v35\n\t" \
  "v_cvt_pk_bf16_f32 v50, v40, v41\n\t" \
  "v_cvt_pk_bf16_f32 v51, v42, v43\n\t" \
  "v_cvt_pk_bf16_f32 v52, v36, v37\n\t" \
  "v_cvt_pk_bf16_f32 v53, v38, v39\n\t" \
  "v_cvt_pk_bf16_f32 v54, v44, v45\n\t" \
  "v_cvt_pk_bf16_f32 v55, v46, v47\n\t" \
  "s_mov_b32 s57, 0\n\t" \
  "buffer_store_dwordx4 v[48:51], v253, s[48:51], s57 offen offset:0 sc1\n\t" \
  "buffer_store_dwordx4 v[52:55], v253, s[48:51], s57 offen offset:512 sc1\n\t" \
  "v_mfma_f32_32x32x16_bf16 a[64:79], v[224:227], v[196:199], a[64:79]\n\t" \
  "ds_read_b64_tr_b16 v[224:225], v246 offset:52864\n\t" \
  "ds_read_b64_tr_b16 v[226:227], v246 offset:61568\n\t" \
  "v_accvgpr_read_b32 v56, a16\n\t" \
  "v_accvgpr_read_b32 v57, a17\n\t" \
  "v_accvgpr_read_b32 v58, a18\n\t" \
  "v_accvgpr_read_b32 v59, a19\n\t" \
  "v_accvgpr_read_b32 v60, a20\n\t" \
  "v_accvgpr_read_b32 v61, a21\n\t" \
  "v_accvgpr_read_b32 v62, a22\n\t" \
  "v_accvgpr_read_b32 v63, a23\n\t" \
  "v_accvgpr_read_b32 v64, a24\n\t" \
  "v_accvgpr_read_b32 v65, a25\n\t" \
  "v_accvgpr_read_b32 v66, a26\n\t" \
  "v_accvgpr_read_b32 v67, a27\n\t" \
  "v_accvgpr_read_b32 v68, a28\n\t" \
  "v_accvgpr_read_b32 v69, a29\n\t" \
  "v_accvgpr_read_b32 v70, a30\n\t" \
  "v_accvgpr_read_b32 v71, a31\n\t" \
  "v_cvt_pk_bf16_f32 v72, v56, v57\n\t" \
  "v_cvt_pk_bf16_f32 v73, v58, v59\n\t" \
  "v_cvt_pk_bf16_f32 v74, v64, v65\n\t" \
  "v_cvt_pk_bf16_f32 v75, v66, v67\n\t" \
  "v_cvt_pk_bf16_f32 v76, v60, v61\n\t" \
  "v_cvt_pk_bf16_f32 v77, v62, v63\n\t" \
  "v_cvt_pk_bf16_f32 v78, v68, v69\n\t" \
  "v_cvt_pk_bf16_f32 v79, v70, v71\n\t" \
  "s_mov_b32 s57, 1024\n\t" \
  "buffer_store_dwordx4 v[72:75], v253, s[48:51], s57 offen offset:0 sc1\n\t" \
  "buffer_store_dwordx4 v[76:79], v253, s[48:51], s57 offen offset:512 sc1\n\t" \
  "v_mfma_f32_32x32x16_bf16 a[80:95], v[228:231], v[196:199], a[80:95]\n\t" \
  "ds_read_b64_tr_b16 v[228:229], v246 offset:52928\n\t" \
  "ds_read_b64_tr_b16 v[230:231], v246 offset:61632\n\t" \
  "v_accvgpr_read_b32 v80, a32\n\t" \
  "v_accvgpr_read_b32 v81, a33\n\t" \
  "v_accvgpr_read_b32 v82, a34\n\t" \
  "v_accvgpr_read_b32 v83, a35\n\t" \
  "v_accvgpr_read_b32 v84, a36\n\t" \
  "v_accvgpr_read_b32 v85, a37\n\t" \
  "v_accvgpr_read_b32 v86, a38\n\t" \
  "v_accvgpr_read_b32 v87, a39\n\t" \
  "v_accvgpr_read_b32 v88, a40\n\t" \
  "v_accvgpr_read_b32 v89, a41\n\t" \
  "v_accvgpr_read_b32 v90, a42\n\t" \
  "v_accvgpr_read_b32 v91, a43\n\t" \
  "v_accvgpr_read_b32 v92, a44\n\t" \
  "v_accvgpr_read_b32 v93, a45\n\t" \
  "v_accvgpr_read_b32 v94, a46\n\t" \
  "v_accvgpr_read_b32 v95, a47\n\t" \
  "v_cvt_pk_bf16_f32 v96, v80, v81\n\t" \
  "v_cvt_pk_bf16_f32 v97, v82, v83\n\t" \
  "v_cvt_pk_bf16_f32 v98, v88, v89\n\t" \
  "v_cvt_pk_bf16_f32 v99, v90, v91\n\t" \
  "v_cvt_pk_bf16_f32 v100, v84, v85\n\t" \
  "v_cvt_pk_bf16_f32 v101, v86, v87\n\t" \
  "v_cvt_pk_bf16_f32 v102, v92, v93\n\t" \
  "v_cvt_pk_bf16_f32 v103, v94, v95\n\t" \
  "s_mov_b32 s57, 2048\n\t" \
  "buffer_store_dwordx4 v[96:99], v253, s[48:51], s57 offen offset:0 sc1\n\t" \
  "buffer_store_dwordx4 v[100:103], v253, s[48:51], s57 offen offset:512 sc1\n\t" \
  "s_waitcnt lgkmcnt(6)\n\t" \
  "v_mfma_f32_32x32x16_bf16 a[96:111], v[208:211], v[196:199], a[96:111]\n\t" \
  "ds_read_b64_tr_b16 v[208:209], v246 offset:52992\n\t" \
  "ds_read_b64_tr_b16 v[210:211], v246 offset:61696\n\t" \
  "v_accvgpr_read_b32 v104, a48\n\t" \
  "v_accvgpr_read_b32 v105, a49\n\t" \
  "v_accvgpr_read_b32 v106, a50\n\t" \
  "v_accvgpr_read_b32 v107, a51\n\t" \
  "v_accvgpr_read_b32 v108, a52\n\t" \
  "v_accvgpr_read_b32 v109, a53\n\t" \
  "v_accvgpr_read_b32 v110, a54\n\t" \
  "v_accvgpr_read_b32 v111, a55\n\t" \
  "v_accvgpr_read_b32 v112, a56\n\t" \
  "v_accvgpr_read_b32 v113, a57\n\t" \
  "v_accvgpr_read_b32 v114, a58\n\t" \
  "v_accvgpr_read_b32 v115, a59\n\t" \
  "v_accvgpr_read_b32 v116, a60\n\t" \
  "v_accvgpr_read_b32 v117, a61\n\t" \
  "v_accvgpr_read_b32 v118, a62\n\t" \
  "v_accvgpr_read_b32 v119, a63\n\t" \
  "v_cvt_pk_bf16_f32 v120, v104, v105\n\t" \
  "v_cvt_pk_bf16_f32 v121, v106, v107\n\t" \
  "v_cvt_pk_bf16_f32 v122, v112, v113\n\t" \
  "v_cvt_pk_bf16_f32 v123, v114, v115\n\t" \
  "v_cvt_pk_bf16_f32 v124, v108, v109\n\t" \
  "v_cvt_pk_bf16_f32 v125, v110, v111\n\t" \
  "v_cvt_pk_bf16_f32 v126, v116, v117\n\t" \
  "v_cvt_pk_bf16_f32 v127, v118, v119\n\t" \
  "s_mov_b32 s57, 3072\n\t" \
  "buffer_store_dwordx4 v[120:123], v253, s[48:51], s57 offen offset:0 sc1\n\t" \
  "buffer_store_dwordx4 v[124:127], v253, s[48:51], s57 offen offset:512 sc1\n\t" \
  "v_mfma_f32_32x32x16_bf16 a[112:127], v[212:215], v[196:199], a[112:127]\n\t" \
  "ds_read_b64_tr_b16 v[212:213], v246 offset:53056\n\t" \
  "ds_read_b64_tr_b16 v[214:215], v246 offset:61760\n\t" \
  "v_accvgpr_read_b32 v128, a64\n\t" \
  "v_accvgpr_read_b32 v129, a65\n\t" \
  "v_accvgpr_read_b32 v130, a66\n\t" \
  "v_accvgpr_read_b32 v131, a67\n\t" \
  "v_accvgpr_read_b32 v132, a68\n\t" \
  "v_accvgpr_read_b32 v133, a69\n\t" \
  "v_accvgpr_read_b32 v134, a70\n\t" \
  "v_accvgpr_read_b32 v135, a71\n\t" \
  "v_accvgpr_read_b32 v136, a72\n\t" \
  "v_accvgpr_read_b32 v137, a73\n\t" \
  "v_accvgpr_read_b32 v138, a74\n\t" \
  "v_accvgpr_read_b32 v139, a75\n\t" \
  "v_accvgpr_read_b32 v140, a76\n\t" \
  "v_accvgpr_read_b32 v141, a77\n\t" \
  "v_accvgpr_read_b32 v142, a78\n\t" \
  "v_accvgpr_read_b32 v143, a79\n\t" \
  "v_cvt_pk_bf16_f32 v144, v128, v129\n\t" \
  "v_cvt_pk_bf16_f32 v145, v130, v131\n\t" \
  "v_cvt_pk_bf16_f32 v146, v136, v137\n\t" \
  "v_cvt_pk_bf16_f32 v147, v138, v139\n\t" \
  "v_cvt_pk_bf16_f32 v148, v132, v133\n\t" \
  "v_cvt_pk_bf16_f32 v149, v134, v135\n\t" \
  "v_cvt_pk_bf16_f32 v150, v140, v141\n\t" \
  "v_cvt_pk_bf16_f32 v151, v142, v143\n\t" \
  "s_mov_b32 s57, 4096\n\t" \
  "buffer_store_dwordx4 v[144:147], v253, s[48:51], s57 offen offset:0 sc1\n\t" \
  "buffer_store_dwordx4 v[148:151], v253, s[48:51], s57 offen offset:512 sc1\n\t" \
  "v_mfma_f32_32x32x16_bf16 a[128:143], v[216:219], v[196:199], a[128:143]\n\t" \
  "ds_read_b64_tr_b16 v[216:217], v246 offset:53120\n\t" \
  "ds_read_b64_tr_b16 v[218:219], v246 offset:61824\n\t" \
  "v_accvgpr_read_b32 v32, a80\n\t" \
  "v_accvgpr_read_b32 v33, a81\n\t" \
  "v_accvgpr_read_b32 v34, a82\n\t" \
  "v_accvgpr_read_b32 v35, a83\n\t" \
  "v_accvgpr_read_b32 v36, a84\n\t" \
  "v_accvgpr_read_b32 v37, a85\n\t" \
  "v_accvgpr_read_b32 v38, a86\n\t" \
  "v_accvgpr_read_b32 v39, a87\n\t" \
  "v_accvgpr_read_b32 v40, a88\n\t" \
  "v_accvgpr_read_b32 v41, a89\n\t" \
  "v_accvgpr_read_b32 v42, a90\n\t" \
  "v_accvgpr_read_b32 v43, a91\n\t" \
  "v_accvgpr_read_b32 v44, a92\n\t" \
  "v_accvgpr_read_b32 v45, a93\n\t" \
  "v_accvgpr_read_b32 v46, a94\n\t" \
  "v_accvgpr_read_b32 v47, a95\n\t" \
  "v_cvt_pk_bf16_f32 v48, v32, v33\n\t" \
  "v_cvt_pk_bf16_f32 v49, v34, v35\n\t" \
  "v_cvt_pk_bf16_f32 v50, v40, v41\n\t" \
  "v_cvt_pk_bf16_f32 v51, v42, v43\n\t" \
  "v_cvt_pk_bf16_f32 v52, v36, v37\n\t" \
  "v_cvt_pk_bf16_f32 v53, v38, v39\n\t" \
  "v_cvt_pk_bf16_f32 v54, v44, v45\n\t" \
  "v_cvt_pk_bf16_f32 v55, v46, v47\n\t" \
  "s_mov_b32 s57, 5120\n\t" \
  "buffer_store_dwordx4 v[48:51], v253, s[48:51], s57 offen offset:0 sc1\n\t" \
  "buffer_store_dwordx4 v[52:55], v253, s[48:51], s57 offen offset:512 sc1\n\t" \
  "s_waitcnt lgkmcnt(6)\n\t" \
  "v_mfma_f32_32x32x16_bf16 a[144:159], v[220:223], v[196:199], a[144:159]\n\t" \
  "ds_read_b64_tr_b16 v[220:221], v246 offset:53184\n\t" \
  "ds_read_b64_tr_b16 v[222:223], v246 offset:61888\n\t" \
  "v_accvgpr_read_b32 v56, a96\n\t" \
  "v_accvgpr_read_b32 v57, a97\n\t" \
  "v_accvgpr_read_b32 v58, a98\n\t" \
  "v_accvgpr_read_b32 v59, a99\n\t" \
  "v_accvgpr_read_b32 v60, a100\n\t" \
  "v_accvgpr_read_b32 v61, a101\n\t" \
  "v_accvgpr_read_b32 v62, a102\n\t" \
  "v_accvgpr_read_b32 v63, a103\n\t" \
  "v_accvgpr_read_b32 v64, a104\n\t" \
  "v_accvgpr_read_b32 v65, a105\n\t" \
  "v_accvgpr_read_b32 v66, a106\n\t" \
  "v_accvgpr_read_b32 v67, a107\n\t" \
  "v_accvgpr_read_b32 v68, a108\n\t" \
  "v_accvgpr_read_b32 v69, a109\n\t" \
  "v_accvgpr_read_b32 v70, a110\n\t" \
  "v_accvgpr_read_b32 v71, a111\n\t" \
  "v_cvt_pk_bf16_f32 v72, v56, v57\n\t" \
  "v_cvt_pk_bf16_f32 v73, v58, v59\n\t" \
  "v_cvt_pk_bf16_f32 v74, v64, v65\n\t" \
  "v_cvt_pk_bf16_f32 v75, v66, v67\n\t" \
  "v_cvt_pk_bf16_f32 v76, v60, v61\n\t" \
  "v_cvt_pk_bf16_f32 v77, v62, v63\n\t" \
  "v_cvt_pk_bf16_f32 v78, v68, v69\n\t" \
  "v_cvt_pk_bf16_f32 v79, v70, v71\n\t" \
  "s_mov_b32 s57, 6144\n\t" \
  "buffer_store_dwordx4 v[72:75], v253, s[48:51], s57 offen offset:0 sc1\n\t" \
  "buffer_store_dwordx4 v[76:79], v253, s[48:51], s57 offen offset:512 sc1\n\t" \
  "v_mfma_f32_32x32x16_bf16 a[160:175], v[224:227], v[196:199], a[160:175]\n\t" \
  "ds_read_b64_tr_b16 v[224:225], v247 offset:6144\n\t" \
  "ds_read_b64_tr_b16 v[226:227], v247 offset:7168\n\t" \
  "v_accvgpr_read_b32 v80, a112\n\t" \
  "v_accvgpr_read_b32 v81, a113\n\t" \
  "v_accvgpr_read_b32 v82, a114\n\t" \
  "v_accvgpr_read_b32 v83, a115\n\t" \
  "v_accvgpr_read_b32 v84, a116\n\t" \
  "v_accvgpr_read_b32 v85, a117\n\t" \
  "v_accvgpr_read_b32 v86, a118\n\t" \
  "v_accvgpr_read_b32 v87, a119\n\t" \
  "v_accvgpr_read_b32 v88, a120\n\t" \
  "v_accvgpr_read_b32 v89, a121\n\t" \
  "v_accvgpr_read_b32 v90, a122\n\t" \
  "v_accvgpr_read_b32 v91, a123\n\t" \
  "v_accvgpr_read_b32 v92, a124\n\t" \
  "v_accvgpr_read_b32 v93, a125\n\t" \
  "v_accvgpr_read_b32 v94, a126\n\t" \
  "v_accvgpr_read_b32 v95, a127\n\t" \
  "v_cvt_pk_bf16_f32 v96, v80, v81\n\t" \
  "v_cvt_pk_bf16_f32 v97, v82, v83\n\t" \
  "v_cvt_pk_bf16_f32 v98, v88, v89\n\t" \
  "v_cvt_pk_bf16_f32 v99, v90, v91\n\t" \
  "v_cvt_pk_bf16_f32 v100, v84, v85\n\t" \
  "v_cvt_pk_bf16_f32 v101, v86, v87\n\t" \
  "v_cvt_pk_bf16_f32 v102, v92, v93\n\t" \
  "v_cvt_pk_bf16_f32 v103, v94, v95\n\t" \
  "s_mov_b32 s57, 7168\n\t" \
  "buffer_store_dwordx4 v[96:99], v253, s[48:51], s57 offen offset:0 sc1\n\t" \
  "buffer_store_dwordx4 v[100:103], v253, s[48:51], s57 offen offset:512 sc1\n\t" \
  "v_mfma_f32_32x32x16_bf16 a[176:191], v[228:231], v[196:199], a[176:191]\n\t" \
  "ds_read_b64_tr_b16 v[228:229], v248 offset:6144\n\t" \
  "ds_read_b64_tr_b16 v[230:231], v248 offset:7168\n\t" \
  "v_accvgpr_read_b32 v104, a128\n\t" \
  "v_accvgpr_read_b32 v105, a129\n\t" \
  "v_accvgpr_read_b32 v106, a130\n\t" \
  "v_accvgpr_read_b32 v107, a131\n\t" \
  "v_accvgpr_read_b32 v108, a132\n\t" \
  "v_accvgpr_read_b32 v109, a133\n\t" \
  "v_accvgpr_read_b32 v110, a134\n\t" \
  "v_accvgpr_read_b32 v111, a135\n\t" \
  "v_accvgpr_read_b32 v112, a136\n\t" \
  "v_accvgpr_read_b32 v113, a137\n\t" \
  "v_accvgpr_read_b32 v114, a138\n\t" \
  "v_accvgpr_read_b32 v115, a139\n\t" \
  "v_accvgpr_read_b32 v116, a140\n\t" \
  "v_accvgpr_read_b32 v117, a141\n\t" \
  "v_accvgpr_read_b32 v118, a142\n\t" \
  "v_accvgpr_read_b32 v119, a143\n\t" \
  "v_cvt_pk_bf16_f32 v120, v104, v105\n\t" \
  "v_cvt_pk_bf16_f32 v121, v106, v107\n\t" \
  "v_cvt_pk_bf16_f32 v122, v112, v113\n\t" \
  "v_cvt_pk_bf16_f32 v123, v114, v115\n\t" \
  "v_cvt_pk_bf16_f32 v124, v108, v109\n\t" \
  "v_cvt_pk_bf16_f32 v125, v110, v111\n\t" \
  "v_cvt_pk_bf16_f32 v126, v116, v117\n\t" \
  "v_cvt_pk_bf16_f32 v127, v118, v119\n\t" \
  "s_mov_b32 s57, 8192\n\t" \
  "buffer_store_dwordx4 v[120:123], v253, s[48:51], s57 offen offset:0 sc1\n\t" \
  "buffer_store_dwordx4 v[124:127], v253, s[48:51], s57 offen offset:512 sc1\n\t" \
  "s_waitcnt lgkmcnt(6)\n\t" \
  "v_mfma_f32_32x32x16_bf16 a[192:207], v[208:211], v[196:199], a[192:207]\n\t" \
  "v_accvgpr_read_b32 v128, a144\n\t" \
  "v_accvgpr_read_b32 v129, a145\n\t" \
  "v_accvgpr_read_b32 v130, a146\n\t" \
  "v_accvgpr_read_b32 v131, a147\n\t" \
  "v_accvgpr_read_b32 v132, a148\n\t" \
  "v_accvgpr_read_b32 v133, a149\n\t" \
  "v_accvgpr_read_b32 v134, a150\n\t" \
  "v_accvgpr_read_b32 v135, a151\n\t" \
  "v_accvgpr_read_b32 v136, a152\n\t" \
  "v_accvgpr_read_b32 v137, a153\n\t" \
  "v_accvgpr_read_b32 v138, a154\n\t" \
  "v_accvgpr_read_b32 v139, a155\n\t" \
  "v_accvgpr_read_b32 v140, a156\n\t" \
  "v_accvgpr_read_b32 v141, a157\n\t" \
  "v_accvgpr_read_b32 v142, a158\n\t" \
  "v_accvgpr_read_b32 v143, a159\n\t" \
  "v_cvt_pk_bf16_f32 v144, v128, v129\n\t" \
  "v_cvt_pk_bf16_f32 v145, v130, v131\n\t" \
  "v_cvt_pk_bf16_f32 v146, v136, v137\n\t" \
  "v_cvt_pk_bf16_f32 v147, v138, v139\n\t" \
  "v_cvt_pk_bf16_f32 v148, v132, v133\n\t" \
  "v_cvt_pk_bf16_f32 v149, v134, v135\n\t" \
  "v_cvt_pk_bf16_f32 v150, v140, v141\n\t" \
  "v_cvt_pk_bf16_f32 v151, v142, v143\n\t" \
  "s_mov_b32 s57, 9216\n\t" \
  "buffer_store_dwordx4 v[144:147], v253, s[48:51], s57 offen offset:0 sc1\n\t" \
  "buffer_store_dwordx4 v[148:151], v253, s[48:51], s57 offen offset:512 sc1\n\t" \
  "v_mfma_f32_32x32x16_bf16 a[208:223], v[212:215], v[196:199], a[208:223]\n\t" \
  "v_accvgpr_read_b32 v32, a160\n\t" \
  "v_accvgpr_read_b32 v33, a161\n\t" \
  "v_accvgpr_read_b32 v34, a162\n\t" \
  "v_accvgpr_read_b32 v35, a163\n\t" \
  "v_accvgpr_read_b32 v36, a164\n\t" \
  "v_accvgpr_read_b32 v37, a165\n\t" \
  "v_accvgpr_read_b32 v38, a166\n\t" \
  "v_accvgpr_read_b32 v39, a167\n\t" \
  "v_accvgpr_read_b32 v40, a168\n\t" \
  "v_accvgpr_read_b32 v41, a169\n\t" \
  "v_accvgpr_read_b32 v42, a170\n\t" \
  "v_accvgpr_read_b32 v43, a171\n\t" \
  "v_accvgpr_read_b32 v44, a172\n\t" \
  "v_accvgpr_read_b32 v45, a173\n\t" \
  "v_accvgpr_read_b32 v46, a174\n\t" \
  "v_accvgpr_read_b32 v47, a175\n\t" \
  "v_cvt_pk_bf16_f32 v48, v32, v33\n\t" \
  "v_cvt_pk_bf16_f32 v49, v34, v35\n\t" \
  "v_cvt_pk_bf16_f32 v50, v40, v41\n\t" \
  "v_cvt_pk_bf16_f32 v51, v42, v43\n\t" \
  "v_cvt_pk_bf16_f32 v52, v36, v37\n\t" \
  "v_cvt_pk_bf16_f32 v53, v38, v39\n\t" \
  "v_cvt_pk_bf16_f32 v54, v44, v45\n\t" \
  "v_cvt_pk_bf16_f32 v55, v46, v47\n\t" \
  "s_mov_b32 s57, 10240\n\t" \
  "buffer_store_dwordx4 v[48:51], v253, s[48:51], s57 offen offset:0 sc1\n\t" \
  "buffer_store_dwordx4 v[52:55], v253, s[48:51], s57 offen offset:512 sc1\n\t" \
  "v_mfma_f32_32x32x16_bf16 a[224:239], v[216:219], v[196:199], a[224:239]\n\t" \
  "v_accvgpr_read_b32 v56, a176\n\t" \
  "v_accvgpr_read_b32 v57, a177\n\t" \
  "v_accvgpr_read_b32 v58, a178\n\t" \
  "v_accvgpr_read_b32 v59, a179\n\t" \
  "v_accvgpr_read_b32 v60, a180\n\t" \
  "v_accvgpr_read_b32 v61, a181\n\t" \
  "v_accvgpr_read_b32 v62, a182\n\t" \
  "v_accvgpr_read_b32 v63, a183\n\t" \
  "v_accvgpr_read_b32 v64, a184\n\t" \
  "v_accvgpr_read_b32 v65, a185\n\t" \
  "v_accvgpr_read_b32 v66, a186\n\t" \
  "v_accvgpr_read_b32 v67, a187\n\t" \
  "v_accvgpr_read_b32 v68, a188\n\t" \
  "v_accvgpr_read_b32 v69, a189\n\t" \
  "v_accvgpr_read_b32 v70, a190\n\t" \
  "v_accvgpr_read_b32 v71, a191\n\t" \
  "v_cvt_pk_bf16_f32 v72, v56, v57\n\t" \
  "v_cvt_pk_bf16_f32 v73, v58, v59\n\t" \
  "v_cvt_pk_bf16_f32 v74, v64, v65\n\t" \
  "v_cvt_pk_bf16_f32 v75, v66, v67\n\t" \
  "v_cvt_pk_bf16_f32 v76, v60, v61\n\t" \
  "v_cvt_pk_bf16_f32 v77, v62, v63\n\t" \
  "v_cvt_pk_bf16_f32 v78, v68, v69\n\t" \
  "v_cvt_pk_bf16_f32 v79, v70, v71\n\t" \
  "s_mov_b32 s57, 11264\n\t" \
  "buffer_store_dwordx4 v[72:75], v253, s[48:51], s57 offen offset:0 sc1\n\t" \
  "buffer_store_dwordx4 v[76:79], v253, s[48:51], s57 offen offset:512 sc1\n\t" \
  "s_waitcnt lgkmcnt(0)\n\t" \
  "v_mfma_f32_32x32x16_bf16 a[240:255], v[220:223], v[196:199], a[240:255]\n\t" \
  "v_accvgpr_read_b32 v80, a192\n\t" \
  "v_accvgpr_read_b32 v81, a193\n\t" \
  "v_accvgpr_read_b32 v82, a194\n\t" \
  "v_accvgpr_read_b32 v83, a195\n\t" \
  "v_accvgpr_read_b32 v84, a196\n\t" \
  "v_accvgpr_read_b32 v85, a197\n\t" \
  "v_accvgpr_read_b32 v86, a198\n\t" \
  "v_accvgpr_read_b32 v87, a199\n\t" \
  "v_accvgpr_read_b32 v88, a200\n\t" \
  "v_accvgpr_read_b32 v89, a201\n\t" \
  "v_accvgpr_read_b32 v90, a202\n\t" \
  "v_accvgpr_read_b32 v91, a203\n\t" \
  "v_accvgpr_read_b32 v92, a204\n\t" \
  "v_accvgpr_read_b32 v93, a205\n\t" \
  "v_accvgpr_read_b32 v94, a206\n\t" \
  "v_accvgpr_read_b32 v95, a207\n\t" \
  "v_cvt_pk_bf16_f32 v96, v80, v81\n\t" \
  "v_cvt_pk_bf16_f32 v97, v82, v83\n\t" \
  "v_cvt_pk_bf16_f32 v98, v88, v89\n\t" \
  "v_cvt_pk_bf16_f32 v99, v90, v91\n\t" \
  "v_cvt_pk_bf16_f32 v100, v84, v85\n\t" \
  "v_cvt_pk_bf16_f32 v101, v86, v87\n\t" \
  "v_cvt_pk_bf16_f32 v102, v92, v93\n\t" \
  "v_cvt_pk_bf16_f32 v103, v94, v95\n\t" \
  "s_mov_b32 s57, 12288\n\t" \
  "buffer_store_dwordx4 v[96:99], v253, s[48:51], s57 offen offset:0 sc1\n\t" \
  "buffer_store_dwordx4 v[100:103], v253, s[48:51], s57 offen offset:512 sc1\n\t" \
  "v_mfma_f32_32x32x16_bf16 v[0:15], v[224:227], v[196:199], v[0:15]\n\t" \
  "v_accvgpr_read_b32 v104, a208\n\t" \
  "v_accvgpr_read_b32 v105, a209\n\t" \
  "v_accvgpr_read_b32 v106, a210\n\t" \
  "v_accvgpr_read_b32 v107, a211\n\t" \
  "v_accvgpr_read_b32 v108, a212\n\t" \
  "v_accvgpr_read_b32 v109, a213\n\t" \
  "v_accvgpr_read_b32 v110, a214\n\t" \
  "v_accvgpr_read_b32 v111, a215\n\t" \
  "v_accvgpr_read_b32 v112, a216\n\t" \
  "v_accvgpr_read_b32 v113, a217\n\t" \
  "v_accvgpr_read_b32 v114, a218\n\t" \
  "v_accvgpr_read_b32 v115, a219\n\t" \
  "v_accvgpr_read_b32 v116, a220\n\t" \
  "v_accvgpr_read_b32 v117, a221\n\t" \
  "v_accvgpr_read_b32 v118, a222\n\t" \
  "v_accvgpr_read_b32 v119, a223\n\t" \
  "v_cvt_pk_bf16_f32 v120, v104, v105\n\t" \
  "v_cvt_pk_bf16_f32 v121, v106, v107\n\t" \
  "v_cvt_pk_bf16_f32 v122, v112, v113\n\t" \
  "v_cvt_pk_bf16_f32 v123, v114, v115\n\t" \
  "v_cvt_pk_bf16_f32 v124, v108, v109\n\t" \
  "v_cvt_pk_bf16_f32 v125, v110, v111\n\t" \
  "v_cvt_pk_bf16_f32 v126, v116, v117\n\t" \
  "v_cvt_pk_bf16_f32 v127, v118, v119\n\t" \
  "s_mov_b32 s57, 13312\n\t" \
  "buffer_store_dwordx4 v[120:123], v253, s[48:51], s57 offen offset:0 sc1\n\t" \
  "buffer_store_dwordx4 v[124:127], v253, s[48:51], s57 offen offset:512 sc1\n\t" \
  "v_mfma_f32_32x32x16_bf16 v[16:31], v[228:231], v[196:199], v[16:31]\n\t" \
  "v_accvgpr_read_b32 v128, a224\n\t" \
  "v_accvgpr_read_b32 v129, a225\n\t" \
  "v_accvgpr_read_b32 v130, a226\n\t" \
  "v_accvgpr_read_b32 v131, a227\n\t" \
  "v_accvgpr_read_b32 v132, a228\n\t" \
  "v_accvgpr_read_b32 v133, a229\n\t" \
  "v_accvgpr_read_b32 v134, a230\n\t" \
  "v_accvgpr_read_b32 v135, a231\n\t" \
  "v_accvgpr_read_b32 v136, a232\n\t" \
  "v_accvgpr_read_b32 v137, a233\n\t" \
  "v_accvgpr_read_b32 v138, a234\n\t" \
  "v_accvgpr_read_b32 v139, a235\n\t" \
  "v_accvgpr_read_b32 v140, a236\n\t" \
  "v_accvgpr_read_b32 v141, a237\n\t" \
  "v_accvgpr_read_b32 v142, a238\n\t" \
  "v_accvgpr_read_b32 v143, a239\n\t" \
  "v_cvt_pk_bf16_f32 v144, v128, v129\n\t" \
  "v_cvt_pk_bf16_f32 v145, v130, v131\n\t" \
  "v_cvt_pk_bf16_f32 v146, v136, v137\n\t" \
  "v_cvt_pk_bf16_f32 v147, v138, v139\n\t" \
  "v_cvt_pk_bf16_f32 v148, v132, v133\n\t" \
  "v_cvt_pk_bf16_f32 v149, v134, v135\n\t" \
  "v_cvt_pk_bf16_f32 v150, v140, v141\n\t" \
  "v_cvt_pk_bf16_f32 v151, v142, v143\n\t" \
  "s_mov_b32 s57, 14336\n\t" \
  "buffer_store_dwordx4 v[144:147], v253, s[48:51], s57 offen offset:0 sc1\n\t" \
  "buffer_store_dwordx4 v[148:151], v253, s[48:51], s57 offen offset:512 sc1\n\t" \
  "s_nop 15\n\t" \
  "s_nop 15\n\t" \
  "v_accvgpr_read_b32 v32, a240\n\t" \
  "v_accvgpr_read_b32 v33, a241\n\t" \
  "v_accvgpr_read_b32 v34, a242\n\t" \
  "v_accvgpr_read_b32 v35, a243\n\t" \
  "v_accvgpr_read_b32 v36, a244\n\t" \
  "v_accvgpr_read_b32 v37, a245\n\t" \
  "v_accvgpr_read_b32 v38, a246\n\t" \
  "v_accvgpr_read_b32 v39, a247\n\t" \
  "v_accvgpr_read_b32 v40, a248\n\t" \
  "v_accvgpr_read_b32 v41, a249\n\t" \
  "v_accvgpr_read_b32 v42, a250\n\t" \
  "v_accvgpr_read_b32 v43, a251\n\t" \
  "v_accvgpr_read_b32 v44, a252\n\t" \
  "v_accvgpr_read_b32 v45, a253\n\t" \
  "v_accvgpr_read_b32 v46, a254\n\t" \
  "v_accvgpr_read_b32 v47, a255\n\t" \
  "v_cvt_pk_bf16_f32 v48, v32, v33\n\t" \
  "v_cvt_pk_bf16_f32 v49, v34, v35\n\t" \
  "v_cvt_pk_bf16_f32 v50, v40, v41\n\t" \
  "v_cvt_pk_bf16_f32 v51, v42, v43\n\t" \
  "v_cvt_pk_bf16_f32 v52, v36, v37\n\t" \
  "v_cvt_pk_bf16_f32 v53, v38, v39\n\t" \
  "v_cvt_pk_bf16_f32 v54, v44, v45\n\t" \
  "v_cvt_pk_bf16_f32 v55, v46, v47\n\t" \
  "s_mov_b32 s57, 15360\n\t" \
  "buffer_store_dwordx4 v[48:51], v253, s[48:51], s57 offen offset:0 sc1\n\t" \
  "buffer_store_dwordx4 v[52:55], v253, s[48:51], s57 offen offset:512 sc1\n\t" \
  "v_cvt_pk_bf16_f32 v72, v0, v1\n\t" \
  "v_cvt_pk_bf16_f32 v73, v2, v3\n\t" \
  "v_cvt_pk_bf16_f32 v74, v8, v9\n\t" \
  "v_cvt_pk_bf16_f32 v75, v10, v11\n\t" \
  "v_cvt_pk_bf16_f32 v76, v4, v5\n\t" \
  "v_cvt_pk_bf16_f32 v77, v6, v7\n\t" \
  "v_cvt_pk_bf16_f32 v78, v12, v13\n\t" \
  "v_cvt_pk_bf16_f32 v79, v14, v15\n\t" \
  "s_mov_b32 s57, 16384\n\t" \
  "buffer_store_dwordx4 v[72:75], v253, s[48:51], s57 offen offset:0 sc1\n\t" \
  "buffer_store_dwordx4 v[76:79], v253, s[48:51], s57 offen offset:512 sc1\n\t" \
  "v_cvt_pk_bf16_f32 v96, v16, v17\n\t" \
  "v_cvt_pk_bf16_f32 v97, v18, v19\n\t" \
  "v_cvt_pk_bf16_f32 v98, v24, v25\n\t" \
  "v_cvt_pk_bf16_f32 v99, v26, v27\n\t" \
  "v_cvt_pk_bf16_f32 v100, v20, v21\n\t" \
  "v_cvt_pk_bf16_f32 v101, v22, v23\n\t" \
  "v_cvt_pk_bf16_f32 v102, v28, v29\n\t" \
  "v_cvt_pk_bf16_f32 v103, v30, v31\n\t" \
  "s_mov_b32 s57, 17408\n\t" \
  "buffer_store_dwordx4 v[96:99], v253, s[48:51], s57 offen offset:0 sc1\n\t" \
  "buffer_store_dwordx4 v[100:103], v253, s[48:51], s57 offen offset:512 sc1\n\t" \
  "s_waitcnt vmcnt(0)\n\t" \
  "s_endpgm\n\t"
#define MLP_ASM_CLOB "v0", "v1", "v2", "v3", "v4", "v5", "v6", "v7", "v8", "v9", "v10", "v11", "v12", "v13", "v14", "v15", "v16", "v17", "v18", "v19", "v20", "v21", "v22", "v23", "v24", "v25", "v26", "v27", "v28", "v29", "v30", "v31", "v32", "v33", "v34", "v35", "v36", "v37", "v38", "v39", "v40", "v41", "v42", "v43", "v44", "v45", "v46", "v47", "v48", "v49", "v50", "v51", "v52", "v53", "v54", "v55", "v56", "v57", "v58", "v59", "v60", "v61", "v62", "v63", "v64", "v65", "v66", "v67", "v68", "v69", "v70", "v71", "v72", "v73", "v74", "v75", "v76", "v77", "v78", "v79", "v80", "v81", "v82", "v83", "v84", "v85", "v86", "v87", "v88", "v89", "v90", "v91", "v92", "v93", "v94", "v95", "v96", "v97", "v98", "v99", "v100", "v101", "v102", "v103", "v104", "v105", "v106", "v107", "v108", "v109", "v110", "v111", "v112", "v113", "v114", "v115", "v116", "v117", "v118", "v119", "v120", "v121", "v122", "v123", "v124", "v125", "v126", "v127", "v128", "v129", "v130", "v131", "v132", "v133", "v134", "v135", "v136", "v137", "v138", "v139", "v140", "v141", "v142", "v143", "v144", "v145", "v146", "v147", "v148", "v149", "v150", "v151", "v152", "v153", "v154", "v155", "v156", "v157", "v158", "v159", "v160", "v161", "v162", "v163", "v164", "v165", "v166", "v167", "v168", "v169", "v170", "v171", "v172", "v173", "v174", "v175", "v176", "v177", "v178", "v179", "v180", "v181", "v182", "v183", "v184", "v185", "v186", "v187", "v188", "v189", "v190", "v191", "v192", "v193", "v194", "v195", "v196", "v197", "v198", "v199", "v200", "v201", "v202", "v203", "v204", "v205", "v206", "v207", "v208", "v209", "v210", "v211", "v212", "v213", "v214", "v215", "v216", "v217", "v218", "v219", "v220", "v221", "v222", "v223", "v224", "v225", "v226", "v227", "v228", "v229", "v230", "v231", "v232", "v233", "v234", "v235", "v236", "v237", "v238", "v239", "v240", "v241", "v242", "v243", "v244", "v245", "v246", "v247", "v248", "v249", "v250", "v251", "v252", "v253", "v254", "v255", "a0", "a1", "a2", "a3", "a4", "a5", "a6", "a7", "a8", "a9", "a10", "a11", "a12", "a13", "a14", "a15", "a16", "a17", "a18", "a19", "a20", "a21", "a22", "a23", "a24", "a25", "a26", "a27", "a28", "a29", "a30", "a31", "a32", "a33", "a34", "a35", "a36", "a37", "a38", "a39", "a40", "a41", "a42", "a43", "a44", "a45", "a46", "a47", "a48", "a49", "a50", "a51", "a52", "a53", "a54", "a55", "a56", "a57", "a58", "a59", "a60", "a61", "a62", "a63", "a64", "a65", "a66", "a67", "a68", "a69", "a70", "a71", "a72", "a73", "a74", "a75", "a76", "a77", "a78", "a79", "a80", "a81", "a82", "a83", "a84", "a85", "a86", "a87", "a88", "a89", "a90", "a91", "a92", "a93", "a94", "a95", "a96", "a97", "a98", "a99", "a100", "a101", "a102", "a103", "a104", "a105", "a106", "a107", "a108", "a109", "a110", "a111", "a112", "a113", "a114", "a115", "a116", "a117", "a118", "a119", "a120", "a121", "a122", "a123", "a124", "a125", "a126", "a127", "a128", "a129", "a130", "a131", "a132", "a133", "a134", "a135", "a136", "a137", "a138", "a139", "a140", "a141", "a142", "a143", "a144", "a145", "a146", "a147", "a148", "a149", "a150", "a151", "a152", "a153", "a154", "a155", "a156", "a157", "a158", "a159", "a160", "a161", "a162", "a163", "a164", "a165", "a166", "a167", "a168", "a169", "a170", "a171", "a172", "a173", "a174", "a175", "a176", "a177", "a178", "a179", "a180", "a181", "a182", "a183", "a184", "a185", "a186", "a187", "a188", "a189", "a190", "a191", "a192", "a193", "a194", "a195", "a196", "a197", "a198", "a199", "a200", "a201", "a202", "a203", "a204", "a205", "a206", "a207", "a208", "a209", "a210", "a211", "a212", "a213", "a214", "a215", "a216", "a217", "a218", "a219", "a220", "a221", "a222", "a223", "a224", "a225", "a226", "a227", "a228", "a229", "a230", "a231", "a232", "a233", "a234", "a235", "a236", "a237", "a238", "a239", "a240", "a241", "a242", "a243", "a244", "a245", "a246", "a247", "a248", "a249", "a250", "a251", "a252", "a253", "a254", "a255", "s40", "s41", "s42", "s43", "s44", "s45", "s46", "s47", "s48", "s49", "s50", "s51", "s52", "s53", "s54", "s55", "s56", "s57", "s58", "s59", "s60", "s61", "s62", "s63", "vcc", "scc", "memory"

constexpr int L2_LDS = 163840;
#define MFMA32(a, b, c) __builtin_amdgcn_mfma_f32_32x32x16_bf16((a), (b), (c), 0, 0, 0)
#define EXP2(x) __builtin_amdgcn_exp2f(x)
#define MAPA 8

#define ASTAMP(i)
DI void attn_role(char* __restrict__ ws, char* smem, int b, int w, const bf16* __restrict__ x, const bf16* __restrict__ wo, const bf16* __restrict__ w3, const bf16* __restrict__ w4) {
  const int head = (b & 7) * 4 + ((b >> 3) & 3), qb = b >> 5;
  const int lane = __builtin_amdgcn_mbcnt_hi(-1, __builtin_amdgcn_mbcnt_lo(-1, 0)), r = lane & 31, h = lane >> 5;
  const char* kv = ws + WS_KV + (size_t)head * KVHEAD_B;
  const int rt = qb * 4 + w;
  const char* qp = ws + WS_Q + ((size_t)head * 16 + rt) * QRT_B;
  u32x4 q0u[4]; unsigned qr[4]; f32x4 km0, km1, km2, km3;
  { const char* a0 = qp + lane * 16; const char* a1 = qp + 4096 + r * 4; const char* a2 = ws + WS_KMAX + head * 64;
    asm volatile("global_load_dwordx4 %0, %1, off" : "=v"(q0u[0]) : "v"(a0) : "memory");
    asm volatile("global_load_dwordx4 %0, %1, off offset:1024" : "=v"(q0u[1]) : "v"(a0) : "memory");
    asm volatile("global_load_dwordx4 %0, %1, off offset:2048" : "=v"(q0u[2]) : "v"(a0) : "memory");
    asm volatile("global_load_dwordx4 %0, %1, off offset:3072" : "=v"(q0u[3]) : "v"(a0) : "memory");
    asm volatile("global_load_dword %0, %1, off" : "=v"(qr[0]) : "v"(a1) : "memory");
    asm volatile("global_load_dword %0, %1, off offset:128" : "=v"(qr[1]) : "v"(a1) : "memory");
    asm volatile("global_load_dword %0, %1, off offset:256" : "=v"(qr[2]) : "v"(a1) : "memory");
    asm volatile("global_load_dword %0, %1, off offset:384" : "=v"(qr[3]) : "v"(a1) : "memory");
    asm volatile("global_load_dwordx4 %0, %1, off" : "=v"(km0) : "v"(a2) : "memory");
    asm volatile("global_load_dwordx4 %0, %1, off offset:16" : "=v"(km1) : "v"(a2) : "memory");
    asm volatile("global_load_dwordx4 %0, %1, off offset:32" : "=v"(km2) : "v"(a2) : "memory");
    asm volatile("global_load_dwordx4 %0, %1, off offset:48" : "=v"(km3) : "v"(a2) : "memory"); }
#pragma unroll
  for (int i = 0; i < 10; ++i) dma16(kv + (w + 4 * i) * 1024 + lane * 16, smem + (w + 4 * i) * 1024);
  asm volatile("s_waitcnt vmcnt(10)" : "+v"(q0u[0]), "+v"(q0u[1]), "+v"(q0u[2]), "+v"(q0u[3]), "+v"(qr[0]), "+v"(qr[1]), "+v"(qr[2]), "+v"(qr[3]), "+v"(km0), "+v"(km1), "+v"(km2), "+v"(km3) :: "memory");
  ASTAMP(1);
  float km = fmaxf(fmaxf(fmaxf(km0[0], km0[1]), fmaxf(km0[2], km0[3])), fmaxf(fmaxf(km1[0], km1[1]), fmaxf(km1[2], km1[3])));
  km = fmaxf(km, fmaxf(fmaxf(fmaxf(km2[0], km2[1]), fmaxf(km2[2], km2[3])), fmaxf(fmaxf(km3[0], km3[1]), fmaxf(km3[2], km3[3]))));
  float mb[4]; bool ok = true;
#pragma unroll
  for (int j = 0; j < 4; ++j) {
    float qs = 0.f;
#pragma unroll
    for (int e = 0; e < 4; ++e) { unsigned u = q0u[j][e]; qs = __builtin_amdgcn_fdot2_f32_bf16(as_bf2(u), as_bf2(u), qs, false); }
    if (h == 0) qs = __builtin_amdgcn_fdot2_f32_bf16(as_bf2(qr[j]), as_bf2(qr[j]), qs, false);
    qs = add_x32(qs);
    mb[j] = __builtin_amdgcn_sqrtf(qs * km) * 1.001f;
    ok = ok && (mb[j] <= 45.0f);
  }
  volatile LDS_AS int* flags = (volatile LDS_AS int*)(LDS_AS char*)(smem + 163808);
  { const int wok = __all(ok) ? 1 : 0; if (lane == 0) flags[w] = wok; }
  barrier();
  const bool fast = __builtin_amdgcn_readfirstlane(flags[0] & flags[1] & flags[2] & flags[3]) != 0;
  u32x4 q1u[4];
#pragma unroll
  for (int j = 0; j < 4; ++j) { q1u[j] = u32x4{0, 0, 0, 0}; if (h == 0) { q1u[j][0] = qr[j]; q1u[j][1] = fast ? (pack2(-mb[j], 0.f) & 0xffffu) : 0u; } }
  const int vl = 9216 + h * 304 + (r < 19 ? r : (r >= 20 ? r - 16 : 3)) * 16;
  f32x16 O[4];
#pragma unroll
  for (int j = 0; j < 4; ++j)
#pragma unroll
    for (int i = 0; i < 16; ++i) O[j][i] = 0.f;
  const unsigned sb = (unsigned)(size_t)(LDS_AS char*)smem;
  ASTAMP(2);
  if (fast) {
    unsigned ka = sb + lane * 16, kra = sb + 8192 + r * 4, va = sb + vl; const unsigned l16 = lane * 16;
    const char* gnext = kv + (w + 40) * 1024; const unsigned lnext = sb + (w + 40) * 1024;
    const char* pf[3];
#pragma unroll
    for (int i = 0; i < 3; ++i) {
      const int t0 = (i * 4 + w) * 64 + lane; int l = b * 689 + t0; l = t0 < 689 ? l : b * 689; l = l < 88128 ? l : 88127;
      pf[i] = l < 41472 ? (const char*)w3 + (size_t)l * 128 : (l < 82944 ? (const char*)w4 + (size_t)(l - 41472) * 128 : (const char*)wo + (size_t)(l - 82944) * 128);
    }
    asm volatile(ATT_ASM_TEXT : "+v"(O[0]), "+v"(O[1]), "+v"(O[2]), "+v"(O[3]), "+v"(ka), "+v"(kra), "+v"(va)
                 : "v"(q0u[0]), "v"(q0u[1]), "v"(q0u[2]), "v"(q0u[3]), "v"(q1u[0]), "v"(q1u[1]), "v"(q1u[2]), "v"(q1u[3]), "v"(l16), "s"(gnext), "s"(lnext),
                   "v"(pf[0]), "v"(pf[1]), "v"(pf[2])
                 : ATT_ASM_CLOB);
    ASTAMP(3);
  } else {
#pragma unroll 1
    for (int i = 10; i < 37; ++i) dma16(kv + (w + 4 * i) * 1024 + lane * 16, smem + (w + 4 * i) * 1024);
    wait_vm<0>();
    barrier();
  }
  if (!fast) {
    const f32x16 zero = {};
#pragma unroll 1
    for (int j = 0; j < 4; ++j) {
      const bf16x8 q0 = __builtin_bit_cast(bf16x8, j == 0 ? q0u[0] : j == 1 ? q0u[1] : j == 2 ? q0u[2] : q0u[3]);
      const bf16x8 q1 = __builtin_bit_cast(bf16x8, j == 0 ? q1u[0] : j == 1 ? q1u[1] : j == 2 ? q1u[2] : q1u[3]);
      f32x16 Oj = zero; float mrun = -3.0e38f;
      for (int t = 0; t < 64; ++t) {
        const char* bb = smem + (t >> 3) * KVBLK_B; const int kt = t & 7;
        bf16x8 kf = *(const bf16x8*)(bb + kt * 1024 + lane * 16);
        u32x4 k1 = {*(const unsigned*)(bb + 8192 + kt * 128 + r * 4), 0x3f80u, 0u, 0u};
        bf16x8 v0 = *(const bf16x8*)(bb + vl + kt * 1216), v1 = *(const bf16x8*)(bb + vl + kt * 1216 + 608);
        f32x16 s = MFMA32(kf, q0, zero);
        s = MFMA32(__builtin_bit_cast(bf16x8, k1), q1, s);
        float mx = s[0];
#pragma unroll
        for (int e = 1; e < 16; ++e) mx = fmaxf(mx, s[e]);
        mx = fmaxf(mx, __shfl_xor(mx, 32));
        const float mn = fmaxf(mrun, mx), al = EXP2(mrun - mn); mrun = mn;
#pragma unroll
        for (int e = 0; e < 16; ++e) Oj[e] *= al;
        u32x4 p0, p1;
#pragma unroll
        for (int e = 0; e < 4; ++e) { p0[e] = pack2(EXP2(s[2 * e] - mn), EXP2(s[2 * e + 1] - mn)); p1[e] = pack2(EXP2(s[8 + 2 * e] - mn), EXP2(s[9 + 2 * e] - mn)); }
        Oj = MFMA32(v0, __builtin_bit_cast(bf16x8, p0), Oj);
        Oj = MFMA32(v1, __builtin_bit_cast(bf16x8, p1), Oj);
      }
      if (j == 0) O[0] = Oj; else if (j == 1) O[1] = Oj; else if (j == 2) O[2] = Oj; else O[3] = Oj;
    }
  }
  const __amdgpu_buffer_rsrc_t ro = __builtin_amdgcn_make_buffer_rsrc((void*)(ws + WS_O), 0, S_ * D_ * 2, 0x00020000);
#pragma unroll
  for (int j = 0; j < 4; ++j) {
    const unsigned lu = f2u(O[j][10]); auto lr = __builtin_amdgcn_permlane32_swap(lu, lu, false, false); const unsigned l0 = lr[0];
    const float il = __builtin_amdgcn_rcpf(u2f(l0));
    unsigned a0 = pack2(O[j][0] * il, O[j][1] * il), a1 = pack2(O[j][2] * il, O[j][3] * il), b0 = pack2(O[j][4] * il, O[j][5] * il), b1 = pack2(O[j][6] * il, O[j][7] * il);
    { auto s0 = __builtin_amdgcn_permlane32_swap(a0, b0, false, false); a0 = s0[0]; b0 = s0[1]; auto s1 = __builtin_amdgcn_permlane32_swap(a1, b1, false, false); a1 = s1[0]; b1 = s1[1]; }
    const int row = rt * 128 + j * 32 + r, rt16 = row >> 4, m = row & 15;
    __builtin_amdgcn_raw_buffer_store_b128(u32x4{a0, a1, b0, b1}, ro, ((rt16 * 18 + (head >> 1)) * 64 + (2 * (head & 1) + h) * 16 + m) * 16, 0, 16);
    if (h == 0) __builtin_amdgcn_raw_buffer_store_b32(pack2(O[j][8] * il, O[j][9] * il), ro, ((rt16 * 18 + 16 + (head >> 4)) * 64 + ((head & 15) >> 2) * 16 + m) * 16 + (head & 3) * 4, 0, 16);
  }
}

DI void arrive(unsigned* cnt, int w, int lane) { if (w == 0 && lane == 0) __hip_atomic_fetch_add(cnt, 1u, __ATOMIC_RELAXED, __HIP_MEMORY_SCOPE_AGENT); }
DI void await(unsigned* cnt, unsigned want, unsigned* tmo, int w, int lane) {
  if (w == 0) {
    unsigned spins = 0;
    while (__builtin_amdgcn_readfirstlane(__hip_atomic_load(cnt, __ATOMIC_RELAXED, __HIP_MEMORY_SCOPE_AGENT)) < want) {
      __builtin_amdgcn_s_sleep(2);
      if (++spins > (1u << 16)) { if (lane == 0) __hip_atomic_store(tmo, 1u, __ATOMIC_RELAXED, __HIP_MEMORY_SCOPE_AGENT); break; }
    }
  }
  barrier();
}
using GW = G<64, 2, 2, 0, 3, 1, 2, 0, 5>;
template <typename P> struct SelfStream { const P& pc; char* smem;
  DI void before(int s) const {
    wait_vm_rt(s < 2 ? 11 : (s + 2 < 9 ? 2 : 0) + (s + 1 < 9 ? 5 : 0) + (s + 3 < 9 ? 2 : 0)); }
  DI void after(int s) const { if (s + 2 < 9) pc.issue(s + 2, smem, 2); if (s + 4 < 9) pc.issue(s + 4, smem, 1); } };
DI void w2_phase(char* __restrict__ ws, char* smem, const bf16* __restrict__ x, const bf16* __restrict__ w2, int rt64, int ct, int w, unsigned* cnt) {
  const int lane = __builtin_amdgcn_mbcnt_hi(-1, __builtin_amdgcn_mbcnt_lo(-1, 0)), r16 = lane & 15, q4 = lane >> 4;
  const Src sc = {ws + WS_H, (S_ + 4) * D_ * 2, (unsigned)(2 + rt64 * 64) * 1152, (const char*)w2, D_ * D_ * 2, (unsigned)(ct * 144 - (ct & 1) * 16) * 2, 1152u, W2_ROT};
  Pieces<GW, GW::L, 1> pc; pc.init(sc, w, lane);
  pc.issue(0, smem); pc.issue(1, smem); pc.issue(2, smem, 1); pc.issue(3, smem, 1);
  const int row0 = rt64 * 64 + (w & 1) * 32 + r16, jt0 = (w >> 1) * 4;
  float mu[2], rs[2]; u32x2 xv[2][5];
#pragma unroll
  for (int i = 0; i < 2; ++i) {
    mu[i] = ((const float*)(ws + WS_STAT))[row0 + 16 * i]; rs[i] = ((const float*)(ws + WS_STAT))[S_ + row0 + 16 * i];
#pragma unroll
    for (int j = 0; j < 5; ++j) xv[i][j] = *(const u32x2*)(x + (size_t)(row0 + 16 * i) * D_ + ct * 144 + (jt0 + j) * 16 + 4 * q4);
  }
  f32x4 acc[2][5];
  gemm_consumer<GW, true, false, false, SelfStream<Pieces<GW, GW::L, 1>>>(acc, w, lane, smem, nullptr, nullptr, ct & 1, SelfStream<Pieces<GW, GW::L, 1>>{pc, smem});
  FST(1);
  const __amdgpu_buffer_rsrc_t rx = __builtin_amdgcn_make_buffer_rsrc((void*)(ws + WS_X1), 0, S_ * D_ * 2, 0x00020000);
  const __amdgpu_buffer_rsrc_t rt = __builtin_amdgcn_make_buffer_rsrc((void*)(ws + WS_ST1), 0, S_ * 128, 0x00020000);
#pragma unroll
  for (int i = 0; i < 2; ++i) {
    const int row = row0 + 16 * i; float ss = 0.f, sq = 0.f;
#pragma unroll
    for (int j = 0; j < 5; ++j) {
      const int J = ct * 9 + jt0 + j; const unsigned x0 = xv[i][j][0], x1 = xv[i][j][1];
      const unsigned v0 = pack2((bf_lo(x0) - mu[i]) * rs[i] + acc[i][j][0], (bf_hi(x0) - mu[i]) * rs[i] + acc[i][j][1]), v1 = pack2((bf_lo(x1) - mu[i]) * rs[i] + acc[i][j][2], (bf_hi(x1) - mu[i]) * rs[i] + acc[i][j][3]);
      __builtin_amdgcn_raw_buffer_store_b64(u32x2{v0, v1}, rx, (((row >> 5) * 36 + J) * 64 + (q4 >> 1) * 32 + (row & 31)) * 16 + (q4 & 1) * 8, 0, 16);
      if (j > 0 || jt0 == 0) {
        const bf16x2 one = {(bf16)1.0f, (bf16)1.0f};
        ss = __builtin_amdgcn_fdot2_f32_bf16(as_bf2(v0), one, ss, false); ss = __builtin_amdgcn_fdot2_f32_bf16(as_bf2(v1), one, ss, false);
        sq = __builtin_amdgcn_fdot2_f32_bf16(as_bf2(v0), as_bf2(v0), sq, false); sq = __builtin_amdgcn_fdot2_f32_bf16(as_bf2(v1), as_bf2(v1), sq, false);
      }
    }
    ss = red_g(ss); sq = red_g(sq);
    if (q4 == 0) __builtin_amdgcn_raw_buffer_store_b64(u32x2{f2u(ss), f2u(sq)}, rt, row * 128 + (ct * 2 + (w >> 1)) * 8, 0, 16);
  }
  wait_vm<0>();
  barrier();
  arrive(cnt, w, lane);
  FST(3);
}
constexpr int FW_STAGE = 64 * 320, FW_NS = 7;
DI void wo_phase(char* __restrict__ ws, char* smem, const bf16* __restrict__ wo, int rt64, int ct, int w, unsigned* cnt) {
  int lane; asm volatile("v_mbcnt_lo_u32_b32 %0, -1, 0\n\tv_mbcnt_hi_u32_b32 %0, -1, %0" : "=v"(lane));
  const int r16 = lane & 15, q4 = lane >> 4, wr = w & 1, jt0 = (w >> 1) * 4, odd = ct & 1;
  FST(0);
  const __amdgpu_buffer_rsrc_t rw = __builtin_amdgcn_make_buffer_rsrc((void*)wo, 0, D_ * D_ * 2, 0x00020000);
  int vo[5], vo8[5];
#pragma unroll
  for (int i = 0; i < 5; ++i) {
    const int q = (w + 4 * i) * 64 + lane, rho = q / 20, k = (rho & ~12) | ((rho & 4) << 1) | ((rho & 8) >> 1), col = (ct * 144 - odd * 16) * 2 + ((q - rho * 20) ^ ((rho >> 1) & 2)) * 16;
    vo[i] = (18 * (k >> 4) + (k & 15)) * 1152 + col; vo8[i] = (18 * (k >> 1) + 16 + (k & 1)) * 1152 + col;
  }
  auto issue = [&](int s) {
#pragma unroll
    for (int i = 0; i < 5; ++i)
      __builtin_amdgcn_raw_ptr_buffer_load_lds(rw, (LDS_AS void*)(smem + (s % FW_NS) * FW_STAGE + (w + 4 * i) * 1024), 16, s < 8 ? vo[i] : vo8[i], s < 8 ? s * 72 * 1152 : 0, 0, 0);
  };
  wait_vm<0>();
  barrier();
  arrive(cnt, w, lane);
  FST(1);
#pragma unroll
  for (int s = 0; s < FW_NS; ++s) issue(s);
  FST(2);
  await(cnt, 32u, (unsigned*)(ws + WS_CNT + 20 * 128), w, lane);
  FST(3);
  const __amdgpu_buffer_rsrc_t ro = __builtin_amdgcn_make_buffer_rsrc((void*)(ws + WS_O), 0, S_ * D_ * 2, 0x00020000);
  const int rt16 = rt64 * 4 + wr * 2;
  u32x4 xf[2][18];
#pragma unroll
  for (int k = 0; k < 18; ++k)
#pragma unroll
    for (int i = 0; i < 2; ++i) xf[i][k] = __builtin_amdgcn_raw_buffer_load_b128(ro, ((rt16 + i) * 18 * 64 + lane) * 16, k * 1024, 16);
  const int wrow = (16 * (q4 >> 1) + 4 * (q4 & 1) + (r16 >> 2)) * 320 + (lane & 3) * 8;
  const int wb0 = wrow + ((odd + jt0) ^ (q4 & 1)) * 32, wb1 = wrow + ((odd + jt0 + 1) ^ (q4 & 1)) * 32;
  f32x4 acc[2][5];
#pragma unroll
  for (int j = 0; j < 5; ++j) { acc[0][j] = f32x4{0, 0, 0, 0}; acc[1][j] = f32x4{0, 0, 0, 0}; }
  const __amdgpu_buffer_rsrc_t rsS = __builtin_amdgcn_make_buffer_rsrc((void*)(ws + WS_S), 0, S_ * D_ * 2, 0x00020000);
  bf16x8 wf[2][5];
  auto rd = [&](int gk) {
    const char* st = smem + ((gk >> 1) % FW_NS) * FW_STAGE + 32 * (gk & 1) * 320;
#pragma unroll
    for (int j = 0; j < 5; ++j) {
      const char* a = st + ((j & 1) ? wb1 + (j - 1) * 32 : wb0 + j * 32);
      s16x4 lo = __builtin_amdgcn_ds_read_tr16_b64_v4i16((LDS_AS s16x4*)a), hi = __builtin_amdgcn_ds_read_tr16_b64_v4i16((LDS_AS s16x4*)(a + 8 * 320));
      wf[gk & 1][j] = __builtin_bit_cast(bf16x8, __builtin_shufflevector(lo, hi, 0, 1, 2, 3, 4, 5, 6, 7));
    }
  };
  wait_vm<0>();
  barrier();
  FST(4);
  rd(0);
#pragma unroll
  for (int gk = 0; gk < 18; ++gk) {
    acc[0][0] = __builtin_amdgcn_mfma_f32_16x16x32_bf16(wf[gk & 1][0], __builtin_bit_cast(bf16x8, xf[0][gk]), acc[0][0], 0, 0, 0);
    SBAR();
    if (gk == 3) { barrier(); issue(7); issue(8); }
    if (gk == 13) { wait_vm<0>(); barrier(); }
    if (gk + 1 < 18) rd(gk + 1);
    SBAR();
#pragma unroll
    for (int j = 0; j < 5; ++j)
#pragma unroll
      for (int i = 0; i < 2; ++i) if (i + j > 0) acc[i][j] = __builtin_amdgcn_mfma_f32_16x16x32_bf16(wf[gk & 1][j], __builtin_bit_cast(bf16x8, xf[i][gk]), acc[i][j], 0, 0, 0);
    SBAR();
  }
#pragma unroll
  for (int i = 0; i < 2; ++i)
#pragma unroll
    for (int j = 0; j < 5; ++j) {
      const int J = ct * 9 + jt0 + j;
      *(u32x2*)(ws + WS_S + ((size_t)((rt16 + i) * 18 + (J >> 1)) * 64 + q4 * 16 + r16) * 16 + (J & 1) * 8) = u32x2{pack2(acc[i][j][0], acc[i][j][1]), pack2(acc[i][j][2], acc[i][j][3])};
    }
  FST(5);
}

__global__ __launch_bounds__(256, 1) void k34_attn_mlp(const bf16* __restrict__ w2, const bf16* __restrict__ w3, const bf16* __restrict__ w4, const bf16* __restrict__ x, const bf16* __restrict__ wo, char* __restrict__ ws) {
  extern __shared__ __attribute__((aligned(16))) char smem[];
  const int bid = blockIdx.x, xg = bid & 7, idx = bid >> 3;
  const int w = __builtin_amdgcn_readfirstlane(threadIdx.x >> 6);
  const int b = (idx >> 1) * 8 + xg;
  if (idx & 1) {
    FST(15);
    attn_role(ws, smem, b, w, x, wo, w3, w4);
    const int head = (b & 7) * 4 + ((b >> 3) & 3), qb = b >> 5;
    wo_phase(ws, smem, wo, qb * 8 + (head >> 2), head & 3, w, (unsigned*)(ws + WS_CNT + (16 + qb) * 128));
  } else {
    const int ix = b >> 3;
    const int rt = xg * 2 + (ix & 1), split = ix >> 1;
    unsigned* cnt = (unsigned*)(ws + WS_CNT + rt * 128);
    FST(15);
    w2_phase(ws, smem, x, w2, rt * 2 + (split >> 2), split & 3, w, cnt);
    const char* x1f = ws + WS_X1 + (size_t)(rt * 4 + w) * 36 * 1024;
    char* part = ws + WS_PART + (size_t)split * S_ * D_ * 2 + (size_t)(rt * 4 + w) * 2 * 18 * 1024;
    const int f0 = split * 576;
    const char* st1 = ws + WS_ST1 + (size_t)(rt * 4 + w) * 32 * 128;
    const char* dbg = st1;
    asm volatile(MLP_ASM_TEXT :: "s"(x1f), "s"(w3), "s"(w4), "s"(part), "s"(f0), "s"(w), "s"(st1), "s"(dbg), "s"(cnt) : MLP_ASM_CLOB);
    __builtin_unreachable();
  }
}

#define K5_T 128
__global__ __launch_bounds__(K5_T) void k5_final(const bf16* __restrict__ x, const char* __restrict__ ws, bf16* __restrict__ out) {
  const int lane = threadIdx.x & 63, blk = blockIdx.x * (K5_T / 64) + (threadIdx.x >> 6);
  const int rt16 = blk / 18, nt = blk - rt16 * 18, q4 = lane >> 4, row = rt16 * 16 + (lane & 15);
  const size_t uo = ((size_t)blk * 64 + lane) * 16;
  u32x4 pv[NSPLIT];
#pragma unroll
  for (int sp = 0; sp < NSPLIT; ++sp) pv[sp] = *(const u32x4*)(ws + WS_PART + (size_t)sp * S_ * D_ * 2 + uo);
  const u32x4 sv = *(const u32x4*)(ws + WS_S + uo);
  u32x2 xv[2], x1v[2];
#pragma unroll
  for (int jj = 0; jj < 2; ++jj) {
    xv[jj] = *(const u32x2*)(x + (size_t)row * D_ + 32 * nt + 16 * jj + 4 * q4);
    x1v[jj] = *(const u32x2*)(ws + WS_X1 + ((size_t)((row >> 5) * 36 + 2 * nt + jj) * 64 + (q4 >> 1) * 32 + (row & 31)) * 16 + (q4 & 1) * 8);
  }
#pragma unroll
  for (int jj = 0; jj < 2; ++jj) {
    const unsigned t0 = x1v[jj][0], t1 = x1v[jj][1], u0 = xv[jj][0], u1 = xv[jj][1], s0 = sv[2 * jj], s1 = sv[2 * jj + 1];
    f32x4 p = {bf_lo(t0), bf_hi(t0), bf_lo(t1), bf_hi(t1)};
#pragma unroll
    for (int sp = 0; sp < NSPLIT; ++sp) { const unsigned a = pv[sp][2 * jj], c = pv[sp][2 * jj + 1]; p += f32x4{bf_lo(a), bf_hi(a), bf_lo(c), bf_hi(c)}; }
    const float xs[4] = {bf_lo(u0), bf_hi(u0), bf_lo(u1), bf_hi(u1)}, ss[4] = {bf_lo(s0), bf_hi(s0), bf_lo(s1), bf_hi(s1)}; float r[4];
#pragma unroll
    for (int e = 0; e < 4; ++e) { const float g = p[e] * __builtin_amdgcn_rcpf(1.0f + __builtin_amdgcn_exp2f(-1.4426950408889634f * p[e])); r[e] = xs[e] + ss[e] * g; }
    *(u32x2*)(out + (size_t)row * D_ + 32 * nt + 16 * jj + 4 * q4) = u32x2{pack2(r[0], r[1]), pack2(r[2], r[3])};
  }
  asm volatile("K5_TAIL:");
}

extern "C" void kernel_launch(void* const* d_in, const int* in_sizes, int n_in, void* d_out, int out_size, void* d_ws, size_t ws_size, hipStream_t stream) {
  const bf16* x = (const bf16*)d_in[0]; const bf16* wqkv = (const bf16*)d_in[1]; const bf16* wo = (const bf16*)d_in[2];
  const bf16* w1 = (const bf16*)d_in[4]; const bf16* w2 = (const bf16*)d_in[5]; const bf16* w3 = (const bf16*)d_in[6]; const bf16* w4 = (const bf16*)d_in[7];
  char* ws = (char*)d_ws;
  constexpr int L128 = G1::STAGE * G1::NS + 3328 + 1024;
  (void)hipFuncSetAttribute((const void*)k1_qkv, hipFuncAttributeMaxDynamicSharedMemorySize, L128);
  (void)hipFuncSetAttribute((const void*)k34_attn_mlp, hipFuncAttributeMaxDynamicSharedMemorySize, L2_LDS);
  k1_qkv<<<256, G1::NW * 64, L128, stream>>>(x, wqkv, w1, w2, w3, w4, wo, ws);
  k34_attn_mlp<<<256, 256, L2_LDS, stream>>>(w2, w3, w4, x, wo, ws);
  k5_final<<<2304 * 64 / K5_T, K5_T, 0, stream>>>(x, ws, (bf16*)d_out);
}
```
